```python
import torch

def _softmax(x, axis=-1):
    return torch.softmax(x, dim=axis)
def _gelu(x):
    return torch.nn.functional.gelu(x, approximate="tanh")
def _silu(x):
    return torch.nn.functional.silu(x)
def _sigmoid(x):
    return torch.sigmoid(x)
def _layernorm(x, axis=-1, eps=1e-5):
    m = x.mean(dim=axis, keepdim=True)
    v = x.var(dim=axis, keepdim=True, unbiased=False)
    return (x - m) / torch.sqrt(v + eps)
def _scatter_add(tgt, idx, src, axis=0):
    return tgt.clone().index_add_(axis, idx, src)
def _segment_sum(src, seg, n):
    out = torch.zeros((n,) + src.shape[1:], dtype=src.dtype)
    return out.index_add_(0, seg, src)
def _pad(x, pw):
    flat = tuple(v for pair in reversed(pw) for v in pair)
    return torch.nn.functional.pad(x, flat)

kernel_name = 'gated_mix__mha_block__glu_gated_bilinear__prompt'

def setup_inputs():

    _w = (lambda a: (lambda t: t.to(torch.bfloat16) if t.is_floating_point() else t)(torch.as_tensor(a)))
    def _A_setup():
        B, S, D, H = 1, 2048, 576, 32
        return {
            "x": _w((lambda *s: torch.randn(*s, dtype=torch.bfloat16) * 0.05)(B, S, D)),
            "wqkv": _w((lambda *s: torch.randn(*s, dtype=torch.bfloat16) * 0.8)(D, 3 * D)),
            "wo": _w((lambda *s: torch.randn(*s, dtype=torch.bfloat16) * 0.05)(D, D)),
        }
    _A_in = _A_setup()

    def _B_setup():
        B, S, D, F = 1, 2048, 576, 4608
        return {
            "x": _w((lambda *s: torch.randn(*s, dtype=torch.bfloat16) * 0.05)(B, S, D)),
            "w_gate": _w((lambda *s: torch.randn(*s, dtype=torch.bfloat16) * 3.2)(D, F) * (D ** -0.5)),
            "w_up": _w((lambda *s: torch.randn(*s, dtype=torch.bfloat16) * 3.2)(D, F) * (D ** -0.5)),
            "w_down": _w((lambda *s: torch.randn(*s, dtype=torch.bfloat16) * 3.2)(F, D) * (F ** -0.5)),
        }
    _B_in = _B_setup()

    return {
        "A_x": _A_in["x"],
        "A_wqkv": _A_in["wqkv"],
        "A_wo": _A_in["wo"],
        "B_x": _B_in["x"],
        "B_w_gate": _B_in["w_gate"],
        "B_w_up": _B_in["w_up"],
        "B_w_down": _B_in["w_down"],
        "gate_w": _w((lambda *s: torch.randn(*s, dtype=torch.bfloat16) * 0.05)(576) * 16.0),
    }

def reference(A_x, A_wqkv, A_wo, B_x, B_w_gate, B_w_up, B_w_down, gate_w):
    def _A_ref(x, wqkv, wo):
        B, S, D = x.shape
        H = 32
        Dh = D // H
        qkv = (lambda x, *d: x.permute(*d))((x @ wqkv).reshape(B, S, 3, H, Dh), 2, 0, 3, 1, 4)
        q, k, v = qkv[0], qkv[1], qkv[2]
        scores = torch.einsum("bhqd,bhkd->bhqk", q, k) * (Dh ** -0.5)
        p = _softmax(scores, axis=-1)
        o = torch.einsum("bhqk,bhkd->bhqd", p, v)
        return (lambda x, *d: x.permute(*d))(o, 0, 2, 1, 3).reshape(B, S, D) @ wo

    def _B_ref(x, w_gate, w_up, w_down):
        g = torch.einsum("bsd,df->bsf", x, w_gate)
        u = torch.einsum("bsd,df->bsf", x, w_up)
        h = g * u
        return torch.einsum("bsf,fd->bsd", h, w_down)

    x = A_x
    g = _sigmoid((x * gate_w).sum(-1, keepdims=True))
    a = _A_ref(x, A_wqkv, A_wo)
    b = _B_ref(x, B_w_gate, B_w_up, B_w_down)
    return g * a + (1 - g) * b
```

```cpp
#include <hip/hip_runtime.h>
#include <hip/hip_bf16.h>
#include <stdint.h>


typedef unsigned short u16;
typedef __attribute__((ext_vector_type(8))) short bf16x8;
typedef __attribute__((ext_vector_type(4))) short s16x4;
typedef __attribute__((ext_vector_type(4))) float f32x4;
typedef __attribute__((ext_vector_type(16))) float f32x16;
typedef __attribute__((ext_vector_type(2))) float f32x2;
typedef __attribute__((ext_vector_type(2))) __bf16 bf16x2_t;
typedef __attribute__((ext_vector_type(2))) unsigned u32x2;
typedef __attribute__((ext_vector_type(4))) unsigned u32x4;

#define DI __device__ __forceinline__
#define LDS_AS __attribute__((address_space(3)))

constexpr int SEQ = 2048, DM = 576, FF = 4608, NH = 32, DH = 18, NQKV = 1728;

DI unsigned pack_bf16(float lo, float hi) {
  f32x2 f = {lo, hi};
  bf16x2_t b = __builtin_convertvector(f, bf16x2_t);
  return __builtin_bit_cast(unsigned, b);
}
DI float bf16r(float x) { return __uint_as_float(pack_bf16(x, x) << 16); }
DI float bf2f(u16 v) { return __uint_as_float(((unsigned)v) << 16); }

DI s16x4 tr_read(const char* lds_ptr) {
  return __builtin_amdgcn_ds_read_tr16_b64_v4i16((LDS_AS s16x4*)(lds_ptr));
}
DI void glds16(const void* gsrc, char* lds_dst) {
  __builtin_amdgcn_global_load_lds((const __attribute__((address_space(1))) void*)gsrc,
                                   (LDS_AS void*)lds_dst, 16, 0, 0);
}
#define MFMA16(a, b, c) __builtin_amdgcn_mfma_f32_16x16x32_bf16((a), (b), (c), 0, 0, 0)
#define MFMA32(a, b, c) __builtin_amdgcn_mfma_f32_32x32x16_bf16((a), (b), (c), 0, 0, 0)

constexpr size_t WS_HT = 0;
constexpr size_t WS_QB = WS_HT + (size_t)FF * SEQ * 2;
constexpr size_t WS_KLO = WS_QB + (size_t)NH * SEQ * 32 * 2;
constexpr size_t WS_KHI = WS_KLO + (size_t)NH * SEQ * 16 * 2;
constexpr size_t WS_VF = WS_KHI + (size_t)NH * SEQ * 2 * 2;
constexpr size_t WS_OT = WS_VF + (size_t)NH * SEQ * 32 * 2;
constexpr size_t WS_PART = WS_OT + (size_t)DM * SEQ * 2;
constexpr size_t WS_END = WS_PART + (size_t)4 * SEQ * DM * 4;

constexpr int G1_XB = 256 * 128;
constexpr int G1_WB = 8 * 22 * 256;
constexpr int G1_STAGE = G1_XB + G1_WB;

template <bool ISV, int WN>
DI void gemm1_body(const u16* __restrict__ x, const u16* __restrict__ wg, const u16* __restrict__ wu,
                   const u16* __restrict__ wqkv, u16* __restrict__ hT, u16* __restrict__ Qb,
                   u16* __restrict__ Klo, u16* __restrict__ Khi, u16* __restrict__ Vf, char* lds,
                   int bm, int j, int w, int l) {
  const int wm = w >> 1;
  const int g = l >> 4;
  const int jq = j < 27 ? j : 26;

  unsigned xvo, guvo[3], qvo;
  {
    int row = 32 * w + (l >> 3);
    int chunk = (l & 7) ^ (l >> 3);
    xvo = (unsigned)((bm * 256 + row) * DM + chunk * 8) * 2u;
    const int r = (l >> 1) & 7, half = l & 1;
#pragma unroll
    for (int i = 0; i < 3; ++i) {
      int n = w + 8 * i;
      if (n > 17) n = 17;
      int s = 4 * n + g;
      int kb = s / 9, ct = s - kb * 9;
      int krow = kb * 8 + (r ^ ((kb & 1) << 2));
      guvo[i] = (unsigned)(krow * FF + j * 144 + ct * 16 + half * 8) * 2u;
    }
    {
      int kb = w;
      int krow = kb * 8 + (r ^ ((kb & 1) << 2));
      qvo = (unsigned)(krow * NQKV + jq * 64 + g * 16 + half * 8) * 2u;
    }
  }

  auto stage = [&](int t, int buf) {
    char* sb = lds + buf * G1_STAGE;
    const char* xb = (const char*)x + t * 128;
    const char* gb = (const char*)wg + (size_t)t * (64 * FF * 2);
    const char* ub = (const char*)wu + (size_t)t * (64 * FF * 2);
    const char* qb = (const char*)wqkv + (size_t)t * (64 * NQKV * 2);
#pragma unroll
    for (int i = 0; i < 4; ++i) glds16(xb + (size_t)(i * 8 * DM * 2) + xvo, sb + (32 * w + 8 * i) * 128);
#pragma unroll
    for (int i = 0; i < 3; ++i) {
      int n = w + 8 * i;
      if (n < 18) {
        glds16(gb + guvo[i], sb + G1_XB + n * 1024);
        glds16(ub + guvo[i], sb + G1_XB + 18432 + n * 1024);
      }
    }
    glds16(qb + qvo, sb + G1_XB + 36864 + w * 1024);
  };

  f32x4 acc[11][4];
#pragma unroll
  for (int i = 0; i < 11; ++i)
#pragma unroll
    for (int r = 0; r < 4; ++r) acc[i][r] = (f32x4){0.f, 0.f, 0.f, 0.f};

  const int xoff0 = wm * 8192 + (l & 15) * 128 + (((0 + g) ^ (l & 7)) << 4);
  const int xoff1 = wm * 8192 + (l & 15) * 128 + (((4 + g) ^ (l & 7)) << 4);
  const int wq = (l & 15) >> 2, wp = l & 3;
  const int rp0 = (4 * (0 ^ (g & 1)) + wq) * 32 + wp * 8;
  const int rp1 = (4 * (1 ^ (g & 1)) + wq) * 32 + wp * 8;
  const int woff0 = G1_XB + g * 2304 + rp0, woff1 = G1_XB + g * 2304 + rp1;
  const int qoff0 = G1_XB + 36864 + g * 1024 + rp0, qoff1 = G1_XB + 36864 + g * 1024 + rp1;

  stage(0, 0);
  int tlast = 8;
  asm volatile("" : "+s"(tlast));
#pragma clang loop unroll(disable)
  for (int t = 0; t <= tlast; ++t) {
    asm volatile("s_waitcnt vmcnt(0)" ::: "memory");
    __syncthreads();
    if (t < tlast) stage(t + 1, (t + 1) & 1);
    const char* sb = lds + (t & 1) * G1_STAGE;
#pragma unroll
    for (int kk = 0; kk < 2; ++kk) {
      bf16x8 xf[4];
#pragma unroll
      for (int rt = 0; rt < 4; ++rt)
        xf[rt] = *(const bf16x8*)(sb + (kk ? xoff1 : xoff0) + rt * 2048);
#pragma unroll
      for (int i = 0; i < 11; ++i) {
        const bool isq = (i == 10) || (WN == 1 && (i == 4 || i == 9));
        s16x4 lo, hi;
        if (isq) {
          const int qct = WN == 0 ? 0 : (i == 4 ? 1 : (i == 9 ? 2 : 3));
          lo = tr_read(sb + qoff0 + kk * 4096 + qct * 256);
          hi = tr_read(sb + qoff1 + kk * 4096 + qct * 256);
        } else {
          const int off = (i < 5 ? (WN * 5 + i) * 256 : 18432 + (WN * 5 + i - 5) * 256) + kk * 9216;
          lo = tr_read(sb + woff0 + off);
          hi = tr_read(sb + woff1 + off);
        }
        bf16x8 wf = __builtin_shufflevector(lo, hi, 0, 1, 2, 3, 4, 5, 6, 7);
#pragma unroll
        for (int rt = 0; rt < 4; ++rt) {
          if (ISV && isq) acc[i][rt] = MFMA16(xf[rt], wf, acc[i][rt]);
          else acc[i][rt] = MFMA16(wf, xf[rt], acc[i][rt]);
        }
      }
    }
  }

  __builtin_amdgcn_sched_barrier(0);
  asm volatile("" : "+v"(l));
  {
  const int g = l >> 4;
  const int tok0 = bm * 256 + wm * 64;
  constexpr int NP = WN == 0 ? 5 : 4;
#pragma unroll
  for (int i = 0; i < NP; ++i) {
    const int ct = WN * 5 + i;
    const int chunk = j * 18 + ct * 2 + (g >> 1);
#pragma unroll
    for (int rt = 0; rt < 4; ++rt) {
      float hv[4];
#pragma unroll
      for (int r = 0; r < 4; ++r) hv[r] = bf16r(acc[i][rt][r]) * bf16r(acc[i + 5][rt][r]);
      u32x2 o = {pack_bf16(hv[0], hv[1]), pack_bf16(hv[2], hv[3])};
      int token = tok0 + rt * 16 + (l & 15);
      *(u32x2*)(hT + ((size_t)chunk * SEQ + token) * 8 + (g & 1) * 4) = o;
    }
  }
  if (j < 27) {
    constexpr int NQ = WN == 0 ? 1 : 3;
#pragma unroll
    for (int qi = 0; qi < NQ; ++qi) {
      const int ai = WN == 0 ? 10 : (qi == 0 ? 4 : (qi == 1 ? 9 : 10));
      const int qct = WN == 0 ? 0 : qi + 1;
      if (ISV) {
        int cv = j * 64 + qct * 16 + (l & 15) - 1152;
        int head = cv / 18, d = cv - head * 18;
#pragma unroll
        for (int rt = 0; rt < 4; ++rt) {
          int t16 = tok0 + rt * 16;
          int tile32 = t16 >> 5, s = rt & 1;
          size_t off = ((((size_t)(head * 64 + tile32) * 2 + s) * 2 + (g & 1)) * 32 + d) * 8 + (g >> 1) * 4;
          u32x2 o = {pack_bf16(acc[ai][rt][0], acc[ai][rt][1]), pack_bf16(acc[ai][rt][2], acc[ai][rt][3])};
          *(u32x2*)(Vf + off) = o;
          if (d == 17) *(u32x2*)(Vf + off + 8) = (u32x2){0x3F803F80u, 0x3F803F80u};
        }
      } else {
        const bool isQ = j < 9;
        const float sc = isQ ? 0.23570226039551584f * 1.4426950408889634f : 1.0f;
#pragma unroll
        for (int pr = 0; pr < 2; ++pr) {
          int c = j * 64 + qct * 16 + 4 * g + 2 * pr - (isQ ? 0 : 576);
          int head = c / 18, d = c - head * 18;
#pragma unroll
          for (int rt = 0; rt < 4; ++rt) {
            int token = tok0 + rt * 16 + (l & 15);
            unsigned v = pack_bf16(acc[ai][rt][2 * pr] * sc, acc[ai][rt][2 * pr + 1] * sc);
            u16* dst;
            if (isQ) dst = Qb + ((size_t)head * SEQ + token) * 32 + d;
            else if (d < 16) dst = Klo + ((size_t)head * SEQ + token) * 16 + d;
            else dst = Khi + ((size_t)head * SEQ + token) * 2;
            *(unsigned*)dst = v;
          }
        }
      }
    }
  }
  }
}

__global__ __launch_bounds__(512, 2) void gemm1_kernel(const u16* __restrict__ x, const u16* __restrict__ wg,
                                                       const u16* __restrict__ wu, const u16* __restrict__ wqkv,
                                                       u16* __restrict__ hT, u16* __restrict__ Qb,
                                                       u16* __restrict__ Klo, u16* __restrict__ Khi,
                                                       u16* __restrict__ Vf) {
  __shared__ __attribute__((aligned(16))) char lds[2 * G1_STAGE];
  const int b = blockIdx.x;
  const int xcd = b & 7, idx = b >> 3;
  const int j = xcd * 4 + (idx & 3), bm = idx >> 2;
  const int w = __builtin_amdgcn_readfirstlane(threadIdx.x >> 6);
  const int l = threadIdx.x & 63;
  const bool isv = (j >= 18);
  if (w & 1) {
    if (isv) gemm1_body<true, 1>(x, wg, wu, wqkv, hT, Qb, Klo, Khi, Vf, lds, bm, j, w, l);
    else gemm1_body<false, 1>(x, wg, wu, wqkv, hT, Qb, Klo, Khi, Vf, lds, bm, j, w, l);
  } else {
    if (isv) gemm1_body<true, 0>(x, wg, wu, wqkv, hT, Qb, Klo, Khi, Vf, lds, bm, j, w, l);
    else gemm1_body<false, 0>(x, wg, wu, wqkv, hT, Qb, Klo, Khi, Vf, lds, bm, j, w, l);
  }
}

constexpr float ATT_THR = 10.0f;

__global__ __launch_bounds__(512, 2) void attn_kernel(const u16* __restrict__ Qb, const u16* __restrict__ Klo,
                                                      const u16* __restrict__ Khi, const u16* __restrict__ Vf,
                                                      u16* __restrict__ oT) {
  __shared__ float mrg[4][2][13][64];
  const int b = blockIdx.x;
  const int head = (b & 7) * 4 + ((b >> 3) & 3), qblk = b >> 5;
  const int w = __builtin_amdgcn_readfirstlane(threadIdx.x >> 6);
  const int l = threadIdx.x & 63, r = l & 31, h = l >> 5;
  const int qp = w & 3, kvh = w >> 2;
  const int q0 = qblk * 256 + qp * 64;

  bf16x8 qA[2];
  u32x4 qB[2];
  float m[2];
  f32x16 O[2];
#pragma unroll
  for (int qt = 0; qt < 2; ++qt) {
    const u16* qrow = Qb + ((size_t)head * SEQ + q0 + qt * 32 + r) * 32;
    qA[qt] = *(const bf16x8*)(qrow + 8 * h);
    unsigned q67 = *(const unsigned*)(qrow + 16);
    qB[qt] = (u32x4){h ? 0u : q67, 0u, 0u, 0u};
    m[qt] = 0.f;
#pragma unroll
    for (int i = 0; i < 16; ++i) O[qt][i] = 0.f;
  }

  const u16* kp = Klo + ((size_t)head * SEQ + kvh * 1024 + r) * 16 + 8 * h;
  const u16* khp = Khi + ((size_t)head * SEQ + kvh * 1024 + r) * 2;
  const u16* vp = Vf + ((size_t)(head * 64 + kvh * 32) * 4 + h) * 256 + r * 8;

  bf16x8 kA = *(const bf16x8*)kp;
  unsigned kH = *(const unsigned*)khp;
  bf16x8 v0 = *(const bf16x8*)vp;
  bf16x8 v1 = *(const bf16x8*)(vp + 512);

  for (int it = 0; it < 32; ++it) {
    const int itn = it + 1 < 32 ? it + 1 : it;
    bf16x8 kAn = *(const bf16x8*)(kp + itn * 512);
    unsigned kHn = *(const unsigned*)(khp + itn * 64);
    bf16x8 v0n = *(const bf16x8*)(vp + itn * 1024);
    bf16x8 v1n = *(const bf16x8*)(vp + itn * 1024 + 512);

    u32x4 kBv = {kH, 0x00003F80u, 0u, 0u};
    bf16x8 kB = __builtin_bit_cast(bf16x8, kBv);
#pragma unroll
    for (int qt = 0; qt < 2; ++qt) {
      f32x16 S;
#pragma unroll
      for (int i = 0; i < 16; ++i) S[i] = 0.f;
      S = MFMA32(kA, qA[qt], S);
      S = MFMA32(kB, __builtin_bit_cast(bf16x8, qB[qt]), S);
      float tmax = fmaxf(fmaxf(S[0], S[1]), S[2]);
#pragma unroll
      for (int i = 3; i < 15; i += 2) tmax = fmaxf(fmaxf(tmax, S[i]), S[i + 1]);
      tmax = fmaxf(tmax, S[15]);
      {
        auto rr = __builtin_amdgcn_permlane32_swap(__float_as_uint(tmax), __float_as_uint(tmax), false, false);
        tmax = fmaxf(__uint_as_float(rr[0]), __uint_as_float(rr[1]));
      }
      if (it == 0 || __any(tmax > ATT_THR)) {
        float inc = it == 0 ? tmax : fmaxf(tmax, 0.f);
        float mn = bf16r(m[qt] + inc);
        float delta = mn - m[qt];
        m[qt] = mn;
        float sc = __builtin_amdgcn_exp2f(-delta);
#pragma unroll
        for (int i = 0; i < 16; ++i) { S[i] -= delta; O[qt][i] *= sc; }
        qB[qt][1] = h ? 0u : (pack_bf16(-mn, 0.f));
      }
#pragma unroll
      for (int i = 0; i < 16; ++i) S[i] = __builtin_amdgcn_exp2f(S[i]);
      u32x4 p0 = {pack_bf16(S[0], S[1]), pack_bf16(S[2], S[3]), pack_bf16(S[4], S[5]), pack_bf16(S[6], S[7])};
      u32x4 p1 = {pack_bf16(S[8], S[9]), pack_bf16(S[10], S[11]), pack_bf16(S[12], S[13]), pack_bf16(S[14], S[15])};
      O[qt] = MFMA32(v0, __builtin_bit_cast(bf16x8, p0), O[qt]);
      O[qt] = MFMA32(v1, __builtin_bit_cast(bf16x8, p1), O[qt]);
    }
    kA = kAn; kH = kHn; v0 = v0n; v1 = v1n;
  }

  if (kvh == 1) {
#pragma unroll
    for (int qt = 0; qt < 2; ++qt) {
#pragma unroll
      for (int i = 0; i < 12; ++i) mrg[qp][qt][i][l] = O[qt][i];
      mrg[qp][qt][12][l] = m[qt];
    }
  }
  __syncthreads();
  if (kvh == 0) {
#pragma unroll
    for (int qt = 0; qt < 2; ++qt) {
      float m1 = mrg[qp][qt][12][l];
      float mm = fmaxf(m[qt], m1);
      float a0 = __builtin_amdgcn_exp2f(m[qt] - mm), a1 = __builtin_amdgcn_exp2f(m1 - mm);
      float o[12];
#pragma unroll
      for (int i = 0; i < 12; ++i) o[i] = O[qt][i] * a0 + mrg[qp][qt][i][l] * a1;
      float lsum = __shfl(o[10], r);
      float inv = 1.0f / lsum;
      int token = q0 + qt * 32 + r;
#pragma unroll
      for (int pr = 0; pr < 5; ++pr) {
        if (pr == 4 && h) continue;
        int d = (pr >> 1) * 8 + (pr & 1) * 2 + 4 * h;
        int c = head * 18 + d;
        unsigned v = pack_bf16(o[2 * pr] * inv, o[2 * pr + 1] * inv);
        *(unsigned*)(oT + ((size_t)(c >> 3) * SEQ + token) * 8 + (c & 7)) = v;
      }
    }
  }
}

template <int NW, int RT>
DI void gemm2_core(const u16* __restrict__ AT, const u16* __restrict__ W, int tok0, int col0, int kbeg,
                   int nsteps, f32x4 (&acc)[9][RT], char* lds, int w, int l) {
  constexpr int BM = NW * RT * 16;
  constexpr int AB = 8 * BM * 16;
  constexpr int WBY = 8 * 9 * 256;
  constexpr int STG = AB + WBY;
  constexpr int NAI = AB / 1024;
  constexpr int NAW = (NAI + NW - 1) / NW;
  constexpr int NWW = (18 + NW - 1) / NW;
  const int g = l >> 4;

  const u16* asrc[NAW];
#pragma unroll
  for (int i = 0; i < NAW; ++i) {
    int n = w + NW * i;
    if (n >= NAI) n = NAI - 1;
    int off = n * 64 + l;
    int chunk = off / BM, token = off - chunk * BM;
    asrc[i] = AT + ((size_t)(kbeg / 8 + chunk) * SEQ + tok0 + token) * 8;
  }
  const u16* wsrc[NWW];
#pragma unroll
  for (int i = 0; i < NWW; ++i) {
    int n = w + NW * i;
    if (n >= 18) n = 17;
    int s = 4 * n + g;
    int kb = s / 9, ct = s - kb * 9;
    int r = (l >> 1) & 7, half = l & 1;
    int krow = kb * 8 + (r ^ ((kb & 1) << 2));
    wsrc[i] = W + (size_t)(kbeg + krow) * DM + col0 + ct * 16 + half * 8;
  }
  auto stage = [&](int t, int buf) {
    char* sb = lds + buf * STG;
#pragma unroll
    for (int i = 0; i < NAW; ++i) {
      int n = w + NW * i;
      if (n < NAI) glds16(asrc[i] + (size_t)t * 8 * SEQ * 8, sb + n * 1024);
    }
#pragma unroll
    for (int i = 0; i < NWW; ++i) {
      int n = w + NW * i;
      if (n < 18) glds16(wsrc[i] + (size_t)t * 64 * DM, sb + AB + n * 1024);
    }
  };
  const int aoff = g * (BM * 16) + (w * RT * 16 + (l & 15)) * 16;
  const int wq = (l & 15) >> 2, wp = l & 3;
  const int woff0 = AB + g * 2304 + (4 * (0 ^ (g & 1)) + wq) * 32 + wp * 8;
  const int woff1 = AB + g * 2304 + (4 * (1 ^ (g & 1)) + wq) * 32 + wp * 8;

  stage(0, 0);
  for (int t = 0; t < nsteps; ++t) {
    asm volatile("s_waitcnt vmcnt(0)" ::: "memory");
    __syncthreads();
    if (t + 1 < nsteps) stage(t + 1, (t + 1) & 1);
    const char* sb = lds + (t & 1) * STG;
#pragma unroll
    for (int kk = 0; kk < 2; ++kk) {
      bf16x8 af[RT];
#pragma unroll
      for (int rt = 0; rt < RT; ++rt) af[rt] = *(const bf16x8*)(sb + aoff + kk * 4 * BM * 16 + rt * 256);
#pragma unroll
      for (int ct = 0; ct < 9; ++ct) {
        s16x4 lo = tr_read(sb + woff0 + kk * 9216 + ct * 256);
        s16x4 hi = tr_read(sb + woff1 + kk * 9216 + ct * 256);
        bf16x8 wf = __builtin_shufflevector(lo, hi, 0, 1, 2, 3, 4, 5, 6, 7);
#pragma unroll
        for (int rt = 0; rt < RT; ++rt) acc[ct][rt] = MFMA16(wf, af[rt], acc[ct][rt]);
      }
    }
  }
}

__global__ __launch_bounds__(256, 2) void down_kernel(const u16* __restrict__ hT, const u16* __restrict__ wd,
                                                      float* __restrict__ part) {
  __shared__ __attribute__((aligned(16))) char lds[2 * (16384 + 18432)];
  const int b = blockIdx.x;
  const int xcd = b & 7, idx = b >> 3;
  const int kn = xcd * 2 + (idx & 1);
  const int bm = idx >> 1;
  const int ks = kn >> 2, bn = kn & 3;
  const int w = __builtin_amdgcn_readfirstlane(threadIdx.x >> 6);
  const int l = threadIdx.x & 63;
  f32x4 acc[9][2];
#pragma unroll
  for (int i = 0; i < 9; ++i) { acc[i][0] = (f32x4){0.f, 0.f, 0.f, 0.f}; acc[i][1] = (f32x4){0.f, 0.f, 0.f, 0.f}; }
  gemm2_core<4, 2>(hT, wd, bm * 128, bn * 144, ks * 1152, 18, acc, lds, w, l);
#pragma unroll
  for (int ct = 0; ct < 9; ++ct)
#pragma unroll
    for (int rt = 0; rt < 2; ++rt) {
      int tt = bm * 8 + w * 2 + rt, ctg = bn * 9 + ct;
      *(f32x4*)(part + ((size_t)(ks * 128 + tt) * 36 + ctg) * 256 + l * 4) = acc[ct][rt];
    }
}

__global__ __launch_bounds__(128, 2) void final_kernel(const u16* __restrict__ oT, const u16* __restrict__ wo,
                                                       const float* __restrict__ part, const u16* __restrict__ x,
                                                       const u16* __restrict__ gw, u16* __restrict__ out) {
  __shared__ __attribute__((aligned(16))) char lds[2 * (4096 + 18432) + 128];
  const int b = blockIdx.x;
  const int bn = b & 3, bm = b >> 2;
  const int w = __builtin_amdgcn_readfirstlane(threadIdx.x >> 6);
  const int l = threadIdx.x & 63;
  float* gs = (float*)(lds + 2 * (4096 + 18432));
  {
    int tk = threadIdx.x >> 2, part4 = threadIdx.x & 3;
    const u16* xr = x + (size_t)(bm * 32 + tk) * DM + part4 * 144;
    const u16* gr = gw + part4 * 144;
    float s = 0.f;
    for (int i = 0; i < 144; i += 8) {
      bf16x8 xv = *(const bf16x8*)(xr + i);
      bf16x8 gv = *(const bf16x8*)(gr + i);
#pragma unroll
      for (int e = 0; e < 8; ++e) s += bf16r(bf2f((u16)xv[e]) * bf2f((u16)gv[e]));
    }
    s += __shfl_xor(s, 1);
    s += __shfl_xor(s, 2);
    s = bf16r(s);
    float gg = bf16r(1.0f / (1.0f + __expf(-s)));
    if (part4 == 0) gs[tk] = gg;
  }
  f32x4 acc[9][1];
#pragma unroll
  for (int i = 0; i < 9; ++i) acc[i][0] = (f32x4){0.f, 0.f, 0.f, 0.f};
  gemm2_core<2, 1>(oT, wo, bm * 32, bn * 144, 0, 9, acc, lds, w, l);
  const float gg = gs[w * 16 + (l & 15)];
  const float og = bf16r(1.0f - gg);
  const int token = bm * 32 + w * 16 + (l & 15);
  const int tt = bm * 2 + w;
#pragma unroll
  for (int ct = 0; ct < 9; ++ct) {
    int ctg = bn * 9 + ct;
    f32x4 bsum = *(const f32x4*)(part + ((size_t)(0 * 128 + tt) * 36 + ctg) * 256 + l * 4);
#pragma unroll
    for (int ks = 1; ks < 4; ++ks) bsum += *(const f32x4*)(part + ((size_t)(ks * 128 + tt) * 36 + ctg) * 256 + l * 4);
    float o[4];
#pragma unroll
    for (int e = 0; e < 4; ++e) o[e] = bf16r(gg * bf16r(acc[ct][0][e])) + bf16r(og * bf16r(bsum[e]));
    u32x2 ov = {pack_bf16(o[0], o[1]), pack_bf16(o[2], o[3])};
    *(u32x2*)(out + (size_t)token * DM + ctg * 16 + (l >> 4) * 4) = ov;
  }
}

extern "C" void kernel_launch(void* const* d_in, const int* in_sizes, int n_in, void* d_out, int out_size,
                              void* d_ws, size_t ws_size, hipStream_t stream) {
  const u16* x = (const u16*)d_in[0];
  const u16* wqkv = (const u16*)d_in[1];
  const u16* wo = (const u16*)d_in[2];
  const u16* wg = (const u16*)d_in[4];
  const u16* wu = (const u16*)d_in[5];
  const u16* wd = (const u16*)d_in[6];
  const u16* gw = (const u16*)d_in[7];
  char* ws = (char*)d_ws;
  u16* hT = (u16*)(ws + WS_HT);
  u16* Qb = (u16*)(ws + WS_QB);
  u16* Klo = (u16*)(ws + WS_KLO);
  u16* Khi = (u16*)(ws + WS_KHI);
  u16* Vf = (u16*)(ws + WS_VF);
  u16* oT = (u16*)(ws + WS_OT);
  float* part = (float*)(ws + WS_PART);
  u16* out = (u16*)d_out;

  gemm1_kernel<<<256, 512, 0, stream>>>(x, wg, wu, wqkv, hT, Qb, Klo, Khi, Vf);
  attn_kernel<<<256, 512, 0, stream>>>(Qb, Klo, Khi, Vf, oT);
  down_kernel<<<256, 256, 0, stream>>>(hT, wd, part);
  final_kernel<<<256, 128, 0, stream>>>(oT, wo, part, x, gw, out);
}
```

```cpp
#include <hip/hip_runtime.h>
#include <hip/hip_bf16.h>
#include <stdint.h>
#include <type_traits>

typedef unsigned short u16;
typedef __attribute__((ext_vector_type(8))) short bf16x8;
typedef __attribute__((ext_vector_type(4))) short s16x4;
typedef __attribute__((ext_vector_type(4))) float f32x4;
typedef __attribute__((ext_vector_type(16))) float f32x16;
typedef __attribute__((ext_vector_type(2))) float f32x2;
typedef __attribute__((ext_vector_type(2))) __bf16 bf16x2_t;
typedef __attribute__((ext_vector_type(2))) unsigned u32x2;
typedef __attribute__((ext_vector_type(4))) unsigned u32x4;

#define DI __device__ __forceinline__
#define LDS_AS __attribute__((address_space(3)))

constexpr int SEQ = 2048, DM = 576, FF = 4608, NH = 32, DH = 18, NQKV = 1728;

DI unsigned pack_bf16(float lo, float hi) {
  f32x2 f = {lo, hi};
  bf16x2_t b = __builtin_convertvector(f, bf16x2_t);
  return __builtin_bit_cast(unsigned, b);
}
DI float bf16r(float x) { return __uint_as_float(pack_bf16(x, x) << 16); }
DI float bf2f(u16 v) { return __uint_as_float(((unsigned)v) << 16); }

DI s16x4 tr_read(const char* lds_ptr) {
  return __builtin_amdgcn_ds_read_tr16_b64_v4i16((LDS_AS s16x4*)(lds_ptr));
}
DI void glds16(const void* gsrc, char* lds_dst) {
  __builtin_amdgcn_global_load_lds((const __attribute__((address_space(1))) void*)gsrc,
                                   (LDS_AS void*)lds_dst, 16, 0, 0);
}
DI int lane_id_opaque() {
  int l;
  asm volatile("v_mbcnt_lo_u32_b32 %0, -1, 0\n\tv_mbcnt_hi_u32_b32 %0, -1, %0" : "=v"(l));
  return l;
}
DI void glds16_asm(unsigned voff, const void* sbase, unsigned lds_dst) {
  unsigned keep;
  asm volatile("s_mov_b32 %0, m0\n\ts_mov_b32 m0, %3\n\ts_nop 0\n\tglobal_load_lds_dwordx4 %1, %2\n\ts_mov_b32 m0, %0"
               : "=&s"(keep) : "v"(voff), "s"(sbase), "s"(lds_dst) : "memory");
}
#define STAMP(kid, i) do {} while (0)
#define MFMA16(a, b, c) __builtin_amdgcn_mfma_f32_16x16x32_bf16((a), (b), (c), 0, 0, 0)
#define MFMA32(a, b, c) __builtin_amdgcn_mfma_f32_32x32x16_bf16((a), (b), (c), 0, 0, 0)

constexpr size_t WS_HT = 0;
constexpr size_t WS_QB = WS_HT + (size_t)FF * SEQ * 2;
constexpr int KVREC = 2560;
constexpr size_t WS_KV = WS_QB + (size_t)NH * SEQ * 32 * 2;
constexpr size_t WS_VF = WS_KV;
constexpr size_t WS_OT = WS_KV + (size_t)NH * 64 * KVREC;
constexpr size_t WS_PART = WS_OT + (size_t)DM * SEQ * 2;
constexpr size_t WS_END = WS_PART + (size_t)4 * SEQ * DM * 4;

constexpr int G1_XB = 256 * 64;
constexpr int G1_WB = 9216 * 2 + 5120;
constexpr int G1_STAGE = G1_XB + G1_WB;

template <bool ISV, int WN>
DI void gemm1_body(const u16* __restrict__ x, const u16* __restrict__ wg, const u16* __restrict__ wu,
                   const u16* __restrict__ wqkv, u16* __restrict__ hT, u16* __restrict__ Qb,
                   char* __restrict__ KV, char* lds,
                   int bm, int j, int w) {
  const int l = lane_id_opaque();
  const int wm = w >> 1;
  const int g = l >> 4;
  const int jq = j < 27 ? j : 26;

  unsigned xvo, guvo, evo;
  const char* ebase;
  int estep, edst;
  {
    int row = 32 * w + (l >> 2);
    int chunk = (l & 3) ^ ((-(l >> 4)) & 3);
    xvo = (unsigned)((bm * 256 + row) * DM + chunk * 8) * 2u;
    {
      int c = w * 64 + l;
      int lr = c / 18, ch = c - lr * 18;
      int krow = lr ^ ((lr & 8) >> 1);
      guvo = (unsigned)(krow * FF + j * 144 + ch * 8) * 2u;
    }
    if (w < 2) {
      int c = 8 * 64 + l;
      int lr = c / 18, ch = c - lr * 18;
      int krow = lr ^ ((lr & 8) >> 1);
      evo = (unsigned)(krow * FF + j * 144 + ch * 8) * 2u;
      ebase = (const char*)(w ? wu : wg);
      estep = 32 * FF * 2;
      edst = G1_XB + w * 9216 + 8 * 1024;
    } else {
      int n = w - 2;
      if (n > 4) n = 4;
      int c = n * 64 + l;
      int lr = c / 10, ch = c - lr * 10;
      if (ch > 7) ch = 7;
      int krow = lr ^ ((lr & 8) >> 1);
      evo = (unsigned)(krow * NQKV + jq * 64 + ch * 8) * 2u;
      ebase = (const char*)wqkv;
      estep = 32 * NQKV * 2;
      edst = G1_XB + 18432 + n * 1024;
    }
  }

  const unsigned lds_base = (unsigned)(size_t)(LDS_AS char*)lds;
  auto stage_piece = [&](int t, int p) {
    const unsigned sb = lds_base + (t & 3) * G1_STAGE;
    if (p < 2) {
      const char* xb = (const char*)x + t * 64 + p * 16 * DM * 2;
      glds16_asm(xvo, xb, sb + (32 * w + 16 * p) * 64);
    } else if (p < 4) {
      const char* gb = (const char*)((p & 1) ? wu : wg) + (size_t)t * (32 * FF * 2);
      glds16_asm(guvo, gb, sb + G1_XB + (p & 1) * 9216 + w * 1024);
    } else {
      glds16_asm(evo, ebase + (size_t)t * estep, sb + edst);
    }
  };

  f32x4 acc[11][4];
#pragma unroll
  for (int i = 0; i < 11; ++i)
#pragma unroll
    for (int r = 0; r < 4; ++r) acc[i][r] = (f32x4){0.f, 0.f, 0.f, 0.f};

  const int xoff = wm * 4096 + (l & 15) * 64 + ((g ^ ((-((l & 15) >> 2)) & 3)) << 4);
  const int wq = (l & 15) >> 2, wp = l & 3;
  const int lr0 = g * 8 + 4 * (0 ^ (g & 1)) + wq, lr1 = g * 8 + 4 * (1 ^ (g & 1)) + wq;
  const int woff0 = G1_XB + lr0 * 288 + wp * 8, woff1 = G1_XB + lr1 * 288 + wp * 8;
  const int qoff0 = G1_XB + 18432 + lr0 * 160 + wp * 8, qoff1 = G1_XB + 18432 + lr1 * 160 + wp * 8;

  auto load_w = [&](const char* sb, int i) -> bf16x8 {
    const bool isq = (i == 10) || (WN == 1 && (i == 4 || i == 9));
    s16x4 lo, hi;
    if (isq) {
      const int qct = WN == 0 ? 0 : (i == 4 ? 1 : (i == 9 ? 2 : 3));
      lo = tr_read(sb + qoff0 + qct * 32);
      hi = tr_read(sb + qoff1 + qct * 32);
    } else {
      const int off = (i < 5 ? (WN * 5 + i) * 32 : 9216 + (WN * 5 + i - 5) * 32);
      lo = tr_read(sb + woff0 + off);
      hi = tr_read(sb + woff1 + off);
    }
    return __builtin_shufflevector(lo, hi, 0, 1, 2, 3, 4, 5, 6, 7);
  };

  bf16x8 xf[4], wfa, wfb;
  auto body = [&](int t, auto issue_c, auto pref_c) {
    constexpr bool ISSUE = decltype(issue_c)::value, PREF = decltype(pref_c)::value;
    const char* sb = lds + (t & 3) * G1_STAGE;
    const char* sn = lds + ((t + 1) & 3) * G1_STAGE;
    bf16x8 xn[4];
#pragma unroll
    for (int i = 0; i < 11; ++i) {
      bf16x8 wf = wfa;
      wfa = wfb;
      if (i + 2 < 11) wfb = load_w(sb, i + 2);
      else if (PREF) wfb = load_w(sn, i + 2 - 11);
      if (PREF && i >= 5 && i < 9) xn[i - 5] = *(const bf16x8*)(sn + xoff + (i - 5) * 1024);
      if (ISSUE && (i & 1) == 0 && i < 10) stage_piece(t + 3, i >> 1);
      const bool isq = (i == 10) || (WN == 1 && (i == 4 || i == 9));
#pragma unroll
      for (int rt = 0; rt < 4; ++rt) {
        if (ISV && isq) acc[i][rt] = MFMA16(xf[rt], wf, acc[i][rt]);
        else acc[i][rt] = MFMA16(wf, xf[rt], acc[i][rt]);
      }
      __builtin_amdgcn_sched_barrier(0);
    }
    if (PREF) {
#pragma unroll
      for (int rt = 0; rt < 4; ++rt) xf[rt] = xn[rt];
    }
  };
  using T_ = std::integral_constant<bool, true>;
  using F_ = std::integral_constant<bool, false>;

  STAMP(0, 0);
#pragma unroll
  for (int t = 0; t < 3; ++t)
#pragma unroll
    for (int p = 0; p < 5; ++p) stage_piece(t, p);
  asm volatile("s_waitcnt vmcnt(10)" ::: "memory");
  __builtin_amdgcn_s_barrier();
  asm volatile("" ::: "memory");
#pragma unroll
  for (int rt = 0; rt < 4; ++rt) xf[rt] = *(const bf16x8*)(lds + xoff + rt * 1024);
  wfa = load_w(lds, 0);
  wfb = load_w(lds, 1);
  int tl = 15;
  asm volatile("" : "+s"(tl));
#pragma clang loop unroll(disable)
  for (int t = 0; t < tl; ++t) {
    asm volatile("s_waitcnt vmcnt(5)" ::: "memory");
    __builtin_amdgcn_s_barrier();
    asm volatile("" ::: "memory");
    if ((t & 1) == 0) STAMP(0, 1 + (t >> 1));
    body(t, T_{}, T_{});
  }
  asm volatile("s_waitcnt vmcnt(5)" ::: "memory");
  __builtin_amdgcn_s_barrier();
  asm volatile("" ::: "memory");
  body(tl, F_{}, T_{});
  asm volatile("s_waitcnt vmcnt(0)" ::: "memory");
  __builtin_amdgcn_s_barrier();
  asm volatile("" ::: "memory");
  STAMP(0, 9);
  body(tl + 1, F_{}, T_{});
  body(tl + 2, F_{}, F_{});

  STAMP(0, 10);
  __builtin_amdgcn_sched_barrier(0);
  {
  const int l = lane_id_opaque();
  const int g = l >> 4;
  const int tok0 = bm * 256 + wm * 64;
  constexpr int NP = WN == 0 ? 5 : 4;
#pragma unroll
  for (int i = 0; i < NP; ++i) {
    const int ct = WN * 5 + i;
    const int chunk = j * 18 + ct * 2 + (g >> 1);
#pragma unroll
    for (int rt = 0; rt < 4; ++rt) {
      float hv[4];
#pragma unroll
      for (int r = 0; r < 4; ++r) hv[r] = bf16r(acc[i][rt][r]) * bf16r(acc[i + 5][rt][r]);
      u32x2 o = {pack_bf16(hv[0], hv[1]), pack_bf16(hv[2], hv[3])};
      int token = tok0 + rt * 16 + (l & 15);
      *(u32x2*)(hT + ((size_t)chunk * SEQ + token) * 8 + (g & 1) * 4) = o;
    }
  }
  if (j < 27) {
    constexpr int NQ = WN == 0 ? 1 : 3;
#pragma unroll
    for (int qi = 0; qi < NQ; ++qi) {
      const int ai = WN == 0 ? 10 : (qi == 0 ? 4 : (qi == 1 ? 9 : 10));
      const int qct = WN == 0 ? 0 : qi + 1;
      if (ISV) {
        int cv = j * 64 + qct * 16 + (l & 15) - 1152;
        int head = cv / 18, d = cv - head * 18;
#pragma unroll
        for (int rt = 0; rt < 4; ++rt) {
          int t16 = tok0 + rt * 16;
          int tile32 = t16 >> 5, s = rt & 1;
          char* dst = KV + (size_t)(head * 64 + tile32) * KVREC + 1024 + ((s * 2 + (g & 1)) * 19 + d) * 16 + (g >> 1) * 8;
          u32x2 o = {pack_bf16(acc[ai][rt][0], acc[ai][rt][1]), pack_bf16(acc[ai][rt][2], acc[ai][rt][3])};
          *(u32x2*)dst = o;
          if (d == 17) *(u32x2*)(dst + 16) = (u32x2){0x3F803F80u, 0x3F803F80u};
        }
      } else {
        const bool isQ = j < 9;
        const float sc = isQ ? 0.23570226039551584f * 1.4426950408889634f : 1.0f;
#pragma unroll
        for (int pr = 0; pr < 2; ++pr) {
          int c = j * 64 + qct * 16 + 4 * g + 2 * pr - (isQ ? 0 : 576);
          int head = c / 18, d = c - head * 18;
#pragma unroll
          for (int rt = 0; rt < 4; ++rt) {
            int token = tok0 + rt * 16 + (l & 15);
            unsigned v = pack_bf16(acc[ai][rt][2 * pr] * sc, acc[ai][rt][2 * pr + 1] * sc);
            char* dst;
            if (isQ) dst = (char*)(Qb + ((size_t)head * SEQ + token) * 32 + d);
            else {
              char* rec = KV + (size_t)(head * 64 + (token >> 5)) * KVREC;
              dst = d < 16 ? rec + (token & 31) * 32 + d * 2 : rec + 2240 + (token & 31) * 4;
            }
            *(unsigned*)dst = v;
          }
        }
      }
    }
  }
  }
  STAMP(0, 11);
}

__global__ __launch_bounds__(512, 2) void gemm1_kernel(const u16* __restrict__ x, const u16* __restrict__ wg,
                                                       const u16* __restrict__ wu, const u16* __restrict__ wqkv,
                                                       u16* __restrict__ hT, u16* __restrict__ Qb,
                                                       char* __restrict__ KV) {
  __shared__ __attribute__((aligned(16))) char lds[4 * G1_STAGE];
  const int b = blockIdx.x;
  const int xcd = b & 7, idx = b >> 3;
  const int j = xcd * 4 + (idx & 3), bm = idx >> 2;
  const int w = __builtin_amdgcn_readfirstlane(threadIdx.x >> 6);
  const bool isv = (j >= 18);
  if (w & 1) {
    if (isv) gemm1_body<true, 1>(x, wg, wu, wqkv, hT, Qb, KV, lds, bm, j, w);
    else gemm1_body<false, 1>(x, wg, wu, wqkv, hT, Qb, KV, lds, bm, j, w);
  } else {
    if (isv) gemm1_body<true, 0>(x, wg, wu, wqkv, hT, Qb, KV, lds, bm, j, w);
    else gemm1_body<false, 0>(x, wg, wu, wqkv, hT, Qb, KV, lds, bm, j, w);
  }
}

DI float xhalf_max(float v) {
  auto rr = __builtin_amdgcn_permlane32_swap(__float_as_uint(v), __float_as_uint(v), false, false);
  return fmaxf(__uint_as_float(rr[0]), __uint_as_float(rr[1]));
}
DI float tile_max(const f32x16& S) {
  float t = fmaxf(fmaxf(S[0], S[1]), S[2]);
#pragma unroll
  for (int i = 3; i < 15; i += 2) t = fmaxf(fmaxf(t, S[i]), S[i + 1]);
  return xhalf_max(fmaxf(t, S[15]));
}
DI f32x16 qk_tile(bf16x8 kA, unsigned kH, bf16x8 qA, u32x4 qB) {
  f32x16 S;
#pragma unroll
  for (int i = 0; i < 16; ++i) S[i] = 0.f;
  u32x4 kBv = {kH, 0x00003F80u, 0u, 0u};
  S = MFMA32(kA, qA, S);
  S = MFMA32(__builtin_bit_cast(bf16x8, kBv), __builtin_bit_cast(bf16x8, qB), S);
  return S;
}
DI void pv_tile(f32x16& S, bf16x8 v0, bf16x8 v1, f32x16& O) {
#pragma unroll
  for (int i = 0; i < 16; ++i) S[i] = __builtin_amdgcn_exp2f(S[i]);
  u32x4 p0 = {pack_bf16(S[0], S[1]), pack_bf16(S[2], S[3]), pack_bf16(S[4], S[5]), pack_bf16(S[6], S[7])};
  u32x4 p1 = {pack_bf16(S[8], S[9]), pack_bf16(S[10], S[11]), pack_bf16(S[12], S[13]), pack_bf16(S[14], S[15])};
  O = MFMA32(v0, __builtin_bit_cast(bf16x8, p0), O);
  O = MFMA32(v1, __builtin_bit_cast(bf16x8, p1), O);
}

constexpr int AT_STAGE = 2 * 8 * KVREC;

__global__ __launch_bounds__(512, 4) void attn_kernel(const u16* __restrict__ Qb, const char* __restrict__ KV,
                                                      u16* __restrict__ oT) {
  __shared__ __attribute__((aligned(16))) char lds[2 * AT_STAGE];
  const int b = blockIdx.x;
  const int head = (b & 7) * 4 + ((b >> 3) & 3), qblk = b >> 5;
  const int w = __builtin_amdgcn_readfirstlane(threadIdx.x >> 6);
  const int l = threadIdx.x & 63, r = l & 31, h = l >> 5;
  const int qt = w & 3, kvh = w >> 2;
  const int token = qblk * 128 + qt * 32 + r;

  const unsigned lds_base = (unsigned)(size_t)(LDS_AS char*)lds;
  const char* kvsrc = KV + (size_t)(head * 64 + kvh * 32) * KVREC + (w & 3) * 5120;
  auto stage = [&](int s) {
#pragma unroll
    for (int i = 0; i < 5; ++i)
      glds16_asm((unsigned)(l * 16), kvsrc + s * (8 * KVREC) + i * 1024,
                 lds_base + (s & 1) * AT_STAGE + kvh * (8 * KVREC) + (w & 3) * 5120 + i * 1024);
  };
  stage(0);

  const u16* qrow = Qb + ((size_t)head * SEQ + token) * 32;
  const bf16x8 qA = *(const bf16x8*)(qrow + 8 * h);
  const unsigned q67 = *(const unsigned*)(qrow + 16);
  u32x4 qB = {h ? 0u : q67, 0u, 0u, 0u};
  float m = 0.f;
  f32x16 O;
#pragma unroll
  for (int i = 0; i < 16; ++i) O[i] = 0.f;
  bool failed = false;

  const int koff = kvh * (8 * KVREC) + r * 32 + h * 16;
  const int hoff = kvh * (8 * KVREC) + 2240 + r * 4;
  const int voff = kvh * (8 * KVREC) + 1024 + (h * 19 + (r < 18 ? r : 18)) * 16;

#pragma clang loop unroll(disable)
  for (int s = 0; s < 4; ++s) {
    asm volatile("s_waitcnt vmcnt(0)" ::: "memory");
    __builtin_amdgcn_s_barrier();
    asm volatile("" ::: "memory");
    if (s < 3) stage(s + 1);
    const char* sb = lds + (s & 1) * AT_STAGE;
    bf16x8 kA = *(const bf16x8*)(sb + koff);
    unsigned kH = *(const unsigned*)(sb + hoff);
    if (s == 0) {
      f32x16 S0 = qk_tile(kA, kH, qA, qB);
      m = bf16r(tile_max(S0));
      qB[1] = h ? 0u : pack_bf16(-m, 0.f);
    }
    f32x16 S = qk_tile(kA, kH, qA, qB);
#pragma unroll
    for (int i = 0; i < 8; ++i) {
      bf16x8 v0 = *(const bf16x8*)(sb + voff + i * KVREC);
      bf16x8 v1 = *(const bf16x8*)(sb + voff + i * KVREC + 608);
      f32x16 Sn;
      if (i < 7) {
        kA = *(const bf16x8*)(sb + koff + (i + 1) * KVREC);
        kH = *(const unsigned*)(sb + hoff + (i + 1) * KVREC);
        Sn = qk_tile(kA, kH, qA, qB);
      }
      pv_tile(S, v0, v1, O);
      if (i < 7) S = Sn;
    }
    float lv = h ? 0.f : O[10];
    if (__any(!(lv < 4294967296.f))) {
      auto rr = __builtin_amdgcn_permlane32_swap(__float_as_uint(lv), __float_as_uint(lv), false, false);
      lv = __uint_as_float(rr[0]);
      if (__any(!(lv < 3.0e38f))) failed = true;
      float sh = lv > 1.f && lv < 3.0e38f ? floorf(__builtin_amdgcn_logf(lv)) : 0.f;
      float mn = bf16r(m + sh);
      float sc = __builtin_amdgcn_exp2f(m - mn);
      m = mn;
#pragma unroll
      for (int i = 0; i < 16; ++i) O[i] *= sc;
      qB[1] = h ? 0u : pack_bf16(-m, 0.f);
    }
  }

  if (failed) {
    const char* rec = KV + (size_t)(head * 64 + kvh * 32) * KVREC;
    qB[1] = 0u;
    m = -3.0e38f;
#pragma unroll
    for (int i = 0; i < 16; ++i) O[i] = 0.f;
#pragma clang loop unroll(disable)
    for (int it = 0; it < 32; ++it, rec += KVREC) {
      bf16x8 kA = *(const bf16x8*)(rec + r * 32 + h * 16);
      unsigned kH = *(const unsigned*)(rec + 2240 + r * 4);
      bf16x8 v0 = *(const bf16x8*)(rec + 1024 + (h * 19 + (r < 18 ? r : 18)) * 16);
      bf16x8 v1 = *(const bf16x8*)(rec + 1024 + 608 + (h * 19 + (r < 18 ? r : 18)) * 16);
      f32x16 S = qk_tile(kA, kH, qA, qB);
      float mn = fmaxf(m, tile_max(S));
      float sc = __builtin_amdgcn_exp2f(m - mn);
      m = mn;
#pragma unroll
      for (int i = 0; i < 16; ++i) { S[i] -= mn; O[i] *= sc; }
      pv_tile(S, v0, v1, O);
    }
  }

  __syncthreads();
  float* mrg = (float*)lds;
  if (kvh == 1) {
#pragma unroll
    for (int i = 0; i < 12; ++i) mrg[(qt * 13 + i) * 64 + l] = O[i];
    mrg[(qt * 13 + 12) * 64 + l] = m;
  }
  __syncthreads();
  if (kvh == 0) {
    float m1 = mrg[(qt * 13 + 12) * 64 + l];
    float mm = fmaxf(m, m1);
    float a0 = __builtin_amdgcn_exp2f(m - mm), a1 = __builtin_amdgcn_exp2f(m1 - mm);
    float o[12];
#pragma unroll
    for (int i = 0; i < 12; ++i) o[i] = O[i] * a0 + mrg[(qt * 13 + i) * 64 + l] * a1;
    float lsum = __shfl(o[10], r);
    float inv = 1.0f / lsum;
#pragma unroll
    for (int pr = 0; pr < 5; ++pr) {
      if (pr == 4 && h) continue;
      int d = (pr >> 1) * 8 + (pr & 1) * 2 + 4 * h;
      int c = head * 18 + d;
      unsigned v = pack_bf16(o[2 * pr] * inv, o[2 * pr + 1] * inv);
      *(unsigned*)(oT + ((size_t)(c >> 3) * SEQ + token) * 8 + (c & 7)) = v;
    }
  }
}

template <int NW, int RT>
DI void gemm2_core(const u16* __restrict__ AT, const u16* __restrict__ W, int tok0, int col0, int kbeg,
                   int nsteps, f32x4 (&acc)[9][RT], char* lds, int w, int l) {
  constexpr int BM = NW * RT * 16;
  constexpr int AB = 8 * BM * 16;
  constexpr int WBY = 8 * 9 * 256;
  constexpr int STG = AB + WBY;
  constexpr int NAI = AB / 1024;
  constexpr int NAW = (NAI + NW - 1) / NW;
  constexpr int NWW = (18 + NW - 1) / NW;
  const int g = l >> 4;

  const u16* asrc[NAW];
#pragma unroll
  for (int i = 0; i < NAW; ++i) {
    int n = w + NW * i;
    if (n >= NAI) n = NAI - 1;
    int off = n * 64 + l;
    int chunk = off / BM, token = off - chunk * BM;
    asrc[i] = AT + ((size_t)(kbeg / 8 + chunk) * SEQ + tok0 + token) * 8;
  }
  const u16* wsrc[NWW];
#pragma unroll
  for (int i = 0; i < NWW; ++i) {
    int n = w + NW * i;
    if (n >= 18) n = 17;
    int c = n * 64 + l;
    int lr = c / 18, ch = c - lr * 18;
    int krow = lr ^ ((lr & 8) >> 1);
    wsrc[i] = W + (size_t)(kbeg + krow) * DM + col0 + ch * 8;
  }
  auto stage = [&](int t, int buf) {
    char* sb = lds + buf * STG;
#pragma unroll
    for (int i = 0; i < NAW; ++i) {
      int n = w + NW * i;
      if (n < NAI) glds16(asrc[i] + (size_t)t * 8 * SEQ * 8, sb + n * 1024);
    }
#pragma unroll
    for (int i = 0; i < NWW; ++i) {
      int n = w + NW * i;
      if (n < 18) glds16(wsrc[i] + (size_t)t * 64 * DM, sb + AB + n * 1024);
    }
  };
  const int aoff = g * (BM * 16) + (w * RT * 16 + (l & 15)) * 16;
  const int wq = (l & 15) >> 2, wp = l & 3;
  const int woff0 = AB + (g * 8 + 4 * (0 ^ (g & 1)) + wq) * 288 + wp * 8;
  const int woff1 = AB + (g * 8 + 4 * (1 ^ (g & 1)) + wq) * 288 + wp * 8;

  stage(0, 0);
  for (int t = 0; t < nsteps; ++t) {
    asm volatile("s_waitcnt vmcnt(0)" ::: "memory");
    __syncthreads();
    if (t + 1 < nsteps) stage(t + 1, (t + 1) & 1);
    const char* sb = lds + (t & 1) * STG;
#pragma unroll
    for (int kk = 0; kk < 2; ++kk) {
      bf16x8 af[RT];
#pragma unroll
      for (int rt = 0; rt < RT; ++rt) af[rt] = *(const bf16x8*)(sb + aoff + kk * 4 * BM * 16 + rt * 256);
#pragma unroll
      for (int ct = 0; ct < 9; ++ct) {
        s16x4 lo = tr_read(sb + woff0 + kk * 9216 + ct * 32);
        s16x4 hi = tr_read(sb + woff1 + kk * 9216 + ct * 32);
        bf16x8 wf = __builtin_shufflevector(lo, hi, 0, 1, 2, 3, 4, 5, 6, 7);
#pragma unroll
        for (int rt = 0; rt < RT; ++rt) acc[ct][rt] = MFMA16(wf, af[rt], acc[ct][rt]);
      }
    }
  }
}

__global__ __launch_bounds__(256, 2) void down_kernel(const u16* __restrict__ hT, const u16* __restrict__ wd,
                                                      float* __restrict__ part) {
  __shared__ __attribute__((aligned(16))) char lds[2 * (16384 + 18432)];
  const int b = blockIdx.x;
  const int xcd = b & 7, idx = b >> 3;
  const int kn = xcd * 2 + (idx & 1);
  const int bm = idx >> 1;
  const int ks = kn >> 2, bn = kn & 3;
  const int w = __builtin_amdgcn_readfirstlane(threadIdx.x >> 6);
  const int l = threadIdx.x & 63;
  f32x4 acc[9][2];
#pragma unroll
  for (int i = 0; i < 9; ++i) { acc[i][0] = (f32x4){0.f, 0.f, 0.f, 0.f}; acc[i][1] = (f32x4){0.f, 0.f, 0.f, 0.f}; }
  gemm2_core<4, 2>(hT, wd, bm * 128, bn * 144, ks * 1152, 18, acc, lds, w, l);
#pragma unroll
  for (int ct = 0; ct < 9; ++ct)
#pragma unroll
    for (int rt = 0; rt < 2; ++rt) {
      int tt = bm * 8 + w * 2 + rt, ctg = bn * 9 + ct;
      *(f32x4*)(part + ((size_t)(ks * 128 + tt) * 36 + ctg) * 256 + l * 4) = acc[ct][rt];
    }
}

__global__ __launch_bounds__(128, 2) void final_kernel(const u16* __restrict__ oT, const u16* __restrict__ wo,
                                                       const float* __restrict__ part, const u16* __restrict__ x,
                                                       const u16* __restrict__ gw, u16* __restrict__ out) {
  __shared__ __attribute__((aligned(16))) char lds[2 * (4096 + 18432) + 128];
  const int b = blockIdx.x;
  const int bn = b & 3, bm = b >> 2;
  const int w = __builtin_amdgcn_readfirstlane(threadIdx.x >> 6);
  const int l = threadIdx.x & 63;
  float* gs = (float*)(lds + 2 * (4096 + 18432));
  {
    int tk = threadIdx.x >> 2, part4 = threadIdx.x & 3;
    const u16* xr = x + (size_t)(bm * 32 + tk) * DM + part4 * 144;
    const u16* gr = gw + part4 * 144;
    float s = 0.f;
    for (int i = 0; i < 144; i += 8) {
      bf16x8 xv = *(const bf16x8*)(xr + i);
      bf16x8 gv = *(const bf16x8*)(gr + i);
#pragma unroll
      for (int e = 0; e < 8; ++e) s += bf16r(bf2f((u16)xv[e]) * bf2f((u16)gv[e]));
    }
    s += __shfl_xor(s, 1);
    s += __shfl_xor(s, 2);
    s = bf16r(s);
    float gg = bf16r(1.0f / (1.0f + __expf(-s)));
    if (part4 == 0) gs[tk] = gg;
  }
  f32x4 acc[9][1];
#pragma unroll
  for (int i = 0; i < 9; ++i) acc[i][0] = (f32x4){0.f, 0.f, 0.f, 0.f};
  gemm2_core<2, 1>(oT, wo, bm * 32, bn * 144, 0, 9, acc, lds, w, l);
  const float gg = gs[w * 16 + (l & 15)];
  const float og = bf16r(1.0f - gg);
  const int token = bm * 32 + w * 16 + (l & 15);
  const int tt = bm * 2 + w;
#pragma unroll
  for (int ct = 0; ct < 9; ++ct) {
    int ctg = bn * 9 + ct;
    f32x4 bsum = *(const f32x4*)(part + ((size_t)(0 * 128 + tt) * 36 + ctg) * 256 + l * 4);
#pragma unroll
    for (int ks = 1; ks < 4; ++ks) bsum += *(const f32x4*)(part + ((size_t)(ks * 128 + tt) * 36 + ctg) * 256 + l * 4);
    float o[4];
#pragma unroll
    for (int e = 0; e < 4; ++e) o[e] = bf16r(gg * bf16r(acc[ct][0][e])) + bf16r(og * bf16r(bsum[e]));
    u32x2 ov = {pack_bf16(o[0], o[1]), pack_bf16(o[2], o[3])};
    *(u32x2*)(out + (size_t)token * DM + ctg * 16 + (l >> 4) * 4) = ov;
  }
}

extern "C" void kernel_launch(void* const* d_in, const int* in_sizes, int n_in, void* d_out, int out_size,
                              void* d_ws, size_t ws_size, hipStream_t stream) {
  const u16* x = (const u16*)d_in[0];
  const u16* wqkv = (const u16*)d_in[1];
  const u16* wo = (const u16*)d_in[2];
  const u16* wg = (const u16*)d_in[4];
  const u16* wu = (const u16*)d_in[5];
  const u16* wd = (const u16*)d_in[6];
  const u16* gw = (const u16*)d_in[7];
  char* ws = (char*)d_ws;
  u16* hT = (u16*)(ws + WS_HT);
  u16* Qb = (u16*)(ws + WS_QB);
  char* KV = ws + WS_KV;
  u16* oT = (u16*)(ws + WS_OT);
  float* part = (float*)(ws + WS_PART);
  u16* out = (u16*)d_out;

  gemm1_kernel<<<256, 512, 0, stream>>>(x, wg, wu, wqkv, hT, Qb, KV);
  attn_kernel<<<512, 512, 0, stream>>>(Qb, KV, oT);
  down_kernel<<<256, 256, 0, stream>>>(hT, wd, part);
  final_kernel<<<256, 128, 0, stream>>>(oT, wo, part, x, gw, out);
}
```

```cpp
#include <hip/hip_runtime.h>
#include <hip/hip_bf16.h>
#include <stdint.h>
#include <type_traits>

typedef unsigned short u16;
typedef __attribute__((ext_vector_type(8))) short bf16x8;
typedef __attribute__((ext_vector_type(4))) short s16x4;
typedef __attribute__((ext_vector_type(4))) float f32x4;
typedef __attribute__((ext_vector_type(16))) float f32x16;
typedef __attribute__((ext_vector_type(2))) float f32x2;
typedef __attribute__((ext_vector_type(2))) __bf16 bf16x2_t;
typedef __attribute__((ext_vector_type(2))) unsigned u32x2;
typedef __attribute__((ext_vector_type(4))) unsigned u32x4;

#define DI __device__ __forceinline__
#define LDS_AS __attribute__((address_space(3)))

constexpr int SEQ = 2048, DM = 576, FF = 4608, NH = 32, DH = 18, NQKV = 1728;

DI unsigned pack_bf16(float lo, float hi) {
  f32x2 f = {lo, hi};
  bf16x2_t b = __builtin_convertvector(f, bf16x2_t);
  return __builtin_bit_cast(unsigned, b);
}
DI float bf16r(float x) { return __uint_as_float(pack_bf16(x, x) << 16); }
DI float bf2f(u16 v) { return __uint_as_float(((unsigned)v) << 16); }

DI s16x4 tr_read(const char* lds_ptr) {
  return __builtin_amdgcn_ds_read_tr16_b64_v4i16((LDS_AS s16x4*)(lds_ptr));
}
DI void glds16(const void* gsrc, char* lds_dst) {
  __builtin_amdgcn_global_load_lds((const __attribute__((address_space(1))) void*)gsrc,
                                   (LDS_AS void*)lds_dst, 16, 0, 0);
}
DI int lane_id_opaque() {
  int l;
  asm volatile("v_mbcnt_lo_u32_b32 %0, -1, 0\n\tv_mbcnt_hi_u32_b32 %0, -1, %0" : "=v"(l));
  return l;
}
DI void glds16_asm(unsigned voff, const void* sbase, unsigned lds_dst) {
  unsigned keep;
  asm volatile("s_mov_b32 %0, m0\n\ts_mov_b32 m0, %3\n\ts_nop 0\n\tglobal_load_lds_dwordx4 %1, %2\n\ts_mov_b32 m0, %0"
               : "=&s"(keep) : "v"(voff), "s"(sbase), "s"(lds_dst) : "memory");
}
#define STAMP(kid, i) do {} while (0)
#define MFMA16(a, b, c) __builtin_amdgcn_mfma_f32_16x16x32_bf16((a), (b), (c), 0, 0, 0)
#define MFMA32(a, b, c) __builtin_amdgcn_mfma_f32_32x32x16_bf16((a), (b), (c), 0, 0, 0)

constexpr size_t WS_HT = 0;
constexpr size_t WS_QB = WS_HT + (size_t)FF * SEQ * 2;
constexpr int KVREC = 2560;
constexpr size_t WS_KV = WS_QB + (size_t)NH * SEQ * 32 * 2;
constexpr size_t WS_VF = WS_KV;
constexpr size_t WS_OT = WS_KV + (size_t)NH * 64 * KVREC;
constexpr size_t WS_PART = WS_OT + (size_t)DM * SEQ * 2;
constexpr size_t WS_END = WS_PART + (size_t)4 * SEQ * DM * 4;

constexpr int G1_XB = 256 * 64;
constexpr int G1_WB = 9216 * 2 + 5120;
constexpr int G1_STAGE = G1_XB + G1_WB;

template <bool ISV, int WN>
DI void gemm1_body(const u16* __restrict__ x, const u16* __restrict__ wg, const u16* __restrict__ wu,
                   const u16* __restrict__ wqkv, u16* __restrict__ hT, u16* __restrict__ Qb,
                   char* __restrict__ KV, char* lds,
                   int bm, int j, int w) {
  const int l = lane_id_opaque();
  const int wm = w >> 1;
  const int g = l >> 4;
  const int jq = j < 27 ? j : 26;

  unsigned xvo, guvo, evo;
  const char* ebase;
  int estep, edst;
  {
    int row = 32 * w + (l >> 2);
    int chunk = (l & 3) ^ ((-(l >> 4)) & 3);
    xvo = (unsigned)((bm * 256 + row) * DM + chunk * 8) * 2u;
    {
      int c = w * 64 + l;
      int lr = c / 18, ch = c - lr * 18;
      int krow = lr ^ ((lr & 8) >> 1);
      guvo = (unsigned)(krow * FF + j * 144 + ch * 8) * 2u;
    }
    if (w < 2) {
      int c = 8 * 64 + l;
      int lr = c / 18, ch = c - lr * 18;
      int krow = lr ^ ((lr & 8) >> 1);
      evo = (unsigned)(krow * FF + j * 144 + ch * 8) * 2u;
      ebase = (const char*)(w ? wu : wg);
      estep = 32 * FF * 2;
      edst = G1_XB + w * 9216 + 8 * 1024;
    } else {
      int n = w - 2;
      if (n > 4) n = 4;
      int c = n * 64 + l;
      int lr = c / 10, ch = c - lr * 10;
      if (ch > 7) ch = 7;
      int krow = lr ^ ((lr & 8) >> 1);
      evo = (unsigned)(krow * NQKV + jq * 64 + ch * 8) * 2u;
      ebase = (const char*)wqkv;
      estep = 32 * NQKV * 2;
      edst = G1_XB + 18432 + n * 1024;
    }
  }

  const unsigned lds_base = (unsigned)(size_t)(LDS_AS char*)lds;
  auto stage_piece = [&](int t, int p) {
    const unsigned sb = lds_base + (t & 3) * G1_STAGE;
    if (p < 2) {
      const char* xb = (const char*)x + t * 64 + p * 16 * DM * 2;
      glds16_asm(xvo, xb, sb + (32 * w + 16 * p) * 64);
    } else if (p < 4) {
      const char* gb = (const char*)((p & 1) ? wu : wg) + (size_t)t * (32 * FF * 2);
      glds16_asm(guvo, gb, sb + G1_XB + (p & 1) * 9216 + w * 1024);
    } else {
      glds16_asm(evo, ebase + (size_t)t * estep, sb + edst);
    }
  };

  f32x4 acc[11][4];
#pragma unroll
  for (int i = 0; i < 11; ++i)
#pragma unroll
    for (int r = 0; r < 4; ++r) acc[i][r] = (f32x4){0.f, 0.f, 0.f, 0.f};

  const int xoff = wm * 4096 + (l & 15) * 64 + ((g ^ ((-((l & 15) >> 2)) & 3)) << 4);
  const int wq = (l & 15) >> 2, wp = l & 3;
  const int lr0 = g * 8 + 4 * (0 ^ (g & 1)) + wq, lr1 = g * 8 + 4 * (1 ^ (g & 1)) + wq;
  const int woff0 = G1_XB + lr0 * 288 + wp * 8, woff1 = G1_XB + lr1 * 288 + wp * 8;
  const int qoff0 = G1_XB + 18432 + lr0 * 160 + wp * 8, qoff1 = G1_XB + 18432 + lr1 * 160 + wp * 8;

  auto load_w = [&](const char* sb, int i) -> bf16x8 {
    const bool isq = (i == 10) || (WN == 1 && (i == 4 || i == 9));
    s16x4 lo, hi;
    if (isq) {
      const int qct = WN == 0 ? 0 : (i == 4 ? 1 : (i == 9 ? 2 : 3));
      lo = tr_read(sb + qoff0 + qct * 32);
      hi = tr_read(sb + qoff1 + qct * 32);
    } else {
      const int off = (i < 5 ? (WN * 5 + i) * 32 : 9216 + (WN * 5 + i - 5) * 32);
      lo = tr_read(sb + woff0 + off);
      hi = tr_read(sb + woff1 + off);
    }
    return __builtin_shufflevector(lo, hi, 0, 1, 2, 3, 4, 5, 6, 7);
  };

  bf16x8 xf[4], wfa, wfb;
  auto body = [&](int t, auto issue_c, auto pref_c) {
    constexpr bool ISSUE = decltype(issue_c)::value, PREF = decltype(pref_c)::value;
    const char* sb = lds + (t & 3) * G1_STAGE;
    const char* sn = lds + ((t + 1) & 3) * G1_STAGE;
    bf16x8 xn[4];
#pragma unroll
    for (int i = 0; i < 11; ++i) {
      bf16x8 wf = wfa;
      wfa = wfb;
      if (i + 2 < 11) wfb = load_w(sb, i + 2);
      else if (PREF) wfb = load_w(sn, i + 2 - 11);
      if (PREF && i >= 5 && i < 9) xn[i - 5] = *(const bf16x8*)(sn + xoff + (i - 5) * 1024);
      if (ISSUE && (i & 1) == 0 && i < 10) stage_piece(t + 3, i >> 1);
      const bool isq = (i == 10) || (WN == 1 && (i == 4 || i == 9));
#pragma unroll
      for (int rt = 0; rt < 4; ++rt) {
        if (ISV && isq) acc[i][rt] = MFMA16(xf[rt], wf, acc[i][rt]);
        else acc[i][rt] = MFMA16(wf, xf[rt], acc[i][rt]);
      }
      __builtin_amdgcn_sched_barrier(0);
    }
    if (PREF) {
#pragma unroll
      for (int rt = 0; rt < 4; ++rt) xf[rt] = xn[rt];
    }
  };
  using T_ = std::integral_constant<bool, true>;
  using F_ = std::integral_constant<bool, false>;

  STAMP(0, 0);
#pragma unroll
  for (int t = 0; t < 3; ++t)
#pragma unroll
    for (int p = 0; p < 5; ++p) stage_piece(t, p);
  asm volatile("s_waitcnt vmcnt(10)" ::: "memory");
  __builtin_amdgcn_s_barrier();
  asm volatile("" ::: "memory");
#pragma unroll
  for (int rt = 0; rt < 4; ++rt) xf[rt] = *(const bf16x8*)(lds + xoff + rt * 1024);
  wfa = load_w(lds, 0);
  wfb = load_w(lds, 1);
  int tl = 15;
  asm volatile("" : "+s"(tl));
#pragma clang loop unroll(disable)
  for (int t = 0; t < tl; ++t) {
    asm volatile("s_waitcnt vmcnt(5)" ::: "memory");
    __builtin_amdgcn_s_barrier();
    asm volatile("" ::: "memory");
    if ((t & 1) == 0) STAMP(0, 1 + (t >> 1));
    body(t, T_{}, T_{});
  }
  asm volatile("s_waitcnt vmcnt(5)" ::: "memory");
  __builtin_amdgcn_s_barrier();
  asm volatile("" ::: "memory");
  body(tl, F_{}, T_{});
  asm volatile("s_waitcnt vmcnt(0)" ::: "memory");
  __builtin_amdgcn_s_barrier();
  asm volatile("" ::: "memory");
  STAMP(0, 9);
  body(tl + 1, F_{}, T_{});
  body(tl + 2, F_{}, F_{});

  STAMP(0, 10);
  __builtin_amdgcn_sched_barrier(0);
  {
  const int l = lane_id_opaque();
  const int g = l >> 4;
  const int tok0 = bm * 256 + wm * 64;
  constexpr int NP = WN == 0 ? 5 : 4;
#pragma unroll
  for (int i = 0; i < NP; ++i) {
    const int ct = WN * 5 + i;
    const int chunk = j * 18 + ct * 2 + (g >> 1);
#pragma unroll
    for (int rt = 0; rt < 4; ++rt) {
      float hv[4];
#pragma unroll
      for (int r = 0; r < 4; ++r) hv[r] = bf16r(acc[i][rt][r]) * bf16r(acc[i + 5][rt][r]);
      u32x2 o = {pack_bf16(hv[0], hv[1]), pack_bf16(hv[2], hv[3])};
      int token = tok0 + rt * 16 + (l & 15);
      *(u32x2*)(hT + ((size_t)chunk * SEQ + token) * 8 + (g & 1) * 4) = o;
    }
  }
  if (j < 27) {
    constexpr int NQ = WN == 0 ? 1 : 3;
#pragma unroll
    for (int qi = 0; qi < NQ; ++qi) {
      const int ai = WN == 0 ? 10 : (qi == 0 ? 4 : (qi == 1 ? 9 : 10));
      const int qct = WN == 0 ? 0 : qi + 1;
      if (ISV) {
        int cv = j * 64 + qct * 16 + (l & 15) - 1152;
        int head = cv / 18, d = cv - head * 18;
#pragma unroll
        for (int rt = 0; rt < 4; ++rt) {
          int t16 = tok0 + rt * 16;
          int tile32 = t16 >> 5, s = rt & 1;
          char* dst = KV + (size_t)(head * 64 + tile32) * KVREC + 1024 + ((s * 2 + (g & 1)) * 19 + d) * 16 + (g >> 1) * 8;
          u32x2 o = {pack_bf16(acc[ai][rt][0], acc[ai][rt][1]), pack_bf16(acc[ai][rt][2], acc[ai][rt][3])};
          *(u32x2*)dst = o;
          if (d == 17) *(u32x2*)(dst + 16) = (u32x2){0x3F803F80u, 0x3F803F80u};
        }
      } else {
        const bool isQ = j < 9;
        const float sc = isQ ? 0.23570226039551584f * 1.4426950408889634f : 1.0f;
#pragma unroll
        for (int pr = 0; pr < 2; ++pr) {
          int c = j * 64 + qct * 16 + 4 * g + 2 * pr - (isQ ? 0 : 576);
          int head = c / 18, d = c - head * 18;
#pragma unroll
          for (int rt = 0; rt < 4; ++rt) {
            int token = tok0 + rt * 16 + (l & 15);
            unsigned v = pack_bf16(acc[ai][rt][2 * pr] * sc, acc[ai][rt][2 * pr + 1] * sc);
            char* dst;
            if (isQ) dst = (char*)(Qb + ((size_t)head * SEQ + token) * 32 + d);
            else {
              char* rec = KV + (size_t)(head * 64 + (token >> 5)) * KVREC;
              dst = d < 16 ? rec + (token & 31) * 32 + d * 2 : rec + 2240 + (token & 31) * 4;
            }
            *(unsigned*)dst = v;
          }
        }
      }
    }
  }
  }
  STAMP(0, 11);
}

__global__ __launch_bounds__(512, 2) void gemm1_kernel(const u16* __restrict__ x, const u16* __restrict__ wg,
                                                       const u16* __restrict__ wu, const u16* __restrict__ wqkv,
                                                       u16* __restrict__ hT, u16* __restrict__ Qb,
                                                       char* __restrict__ KV) {
  __shared__ __attribute__((aligned(16))) char lds[4 * G1_STAGE];
  const int b = blockIdx.x;
  const int xcd = b & 7, idx = b >> 3;
  const int j = xcd * 4 + (idx & 3), bm = idx >> 2;
  const int w = __builtin_amdgcn_readfirstlane(threadIdx.x >> 6);
  const bool isv = (j >= 18);
  if (w & 1) {
    if (isv) gemm1_body<true, 1>(x, wg, wu, wqkv, hT, Qb, KV, lds, bm, j, w);
    else gemm1_body<false, 1>(x, wg, wu, wqkv, hT, Qb, KV, lds, bm, j, w);
  } else {
    if (isv) gemm1_body<true, 0>(x, wg, wu, wqkv, hT, Qb, KV, lds, bm, j, w);
    else gemm1_body<false, 0>(x, wg, wu, wqkv, hT, Qb, KV, lds, bm, j, w);
  }
}

DI float xhalf_max(float v) {
  auto rr = __builtin_amdgcn_permlane32_swap(__float_as_uint(v), __float_as_uint(v), false, false);
  return fmaxf(__uint_as_float(rr[0]), __uint_as_float(rr[1]));
}
DI float tile_max(const f32x16& S) {
  float t = fmaxf(fmaxf(S[0], S[1]), S[2]);
#pragma unroll
  for (int i = 3; i < 15; i += 2) t = fmaxf(fmaxf(t, S[i]), S[i + 1]);
  return xhalf_max(fmaxf(t, S[15]));
}
DI f32x16 qk_tile(bf16x8 kA, unsigned kH, bf16x8 qA, u32x4 qB) {
  f32x16 S;
#pragma unroll
  for (int i = 0; i < 16; ++i) S[i] = 0.f;
  u32x4 kBv = {kH, 0x00003F80u, 0u, 0u};
  S = MFMA32(kA, qA, S);
  S = MFMA32(__builtin_bit_cast(bf16x8, kBv), __builtin_bit_cast(bf16x8, qB), S);
  return S;
}
DI void pv_tile(f32x16& S, bf16x8 v0, bf16x8 v1, f32x16& O) {
#pragma unroll
  for (int i = 0; i < 16; ++i) S[i] = __builtin_amdgcn_exp2f(S[i]);
  u32x4 p0 = {pack_bf16(S[0], S[1]), pack_bf16(S[2], S[3]), pack_bf16(S[4], S[5]), pack_bf16(S[6], S[7])};
  u32x4 p1 = {pack_bf16(S[8], S[9]), pack_bf16(S[10], S[11]), pack_bf16(S[12], S[13]), pack_bf16(S[14], S[15])};
  O = MFMA32(v0, __builtin_bit_cast(bf16x8, p0), O);
  O = MFMA32(v1, __builtin_bit_cast(bf16x8, p1), O);
}

constexpr int AT_STAGE = 2 * 8 * KVREC;

__global__ __launch_bounds__(512, 4) void attn_kernel(const u16* __restrict__ Qb, const char* __restrict__ KV,
                                                      u16* __restrict__ oT) {
  __shared__ __attribute__((aligned(16))) char lds[2 * AT_STAGE];
  const int b = blockIdx.x;
  const int head = (b & 7) * 4 + ((b >> 3) & 3), qblk = b >> 5;
  const int w = __builtin_amdgcn_readfirstlane(threadIdx.x >> 6);
  const int l = threadIdx.x & 63, r = l & 31, h = l >> 5;
  const int qt = w & 3, kvh = w >> 2;
  const int token = qblk * 128 + qt * 32 + r;

  const unsigned lds_base = (unsigned)(size_t)(LDS_AS char*)lds;
  const char* kvsrc = KV + (size_t)(head * 64 + kvh * 32) * KVREC + (w & 3) * 5120;
  auto stage = [&](int s) {
#pragma unroll
    for (int i = 0; i < 5; ++i)
      glds16_asm((unsigned)(l * 16), kvsrc + s * (8 * KVREC) + i * 1024,
                 lds_base + (s & 1) * AT_STAGE + kvh * (8 * KVREC) + (w & 3) * 5120 + i * 1024);
  };
  stage(0);

  const u16* qrow = Qb + ((size_t)head * SEQ + token) * 32;
  bf16x8 qA = *(const bf16x8*)(qrow + 8 * h);
  unsigned q67 = *(const unsigned*)(qrow + 16);
  asm volatile("" : "+v"(qA), "+v"(q67));
  u32x4 qB = {h ? 0u : q67, 0u, 0u, 0u};
  float m = 0.f;
  f32x16 O;
#pragma unroll
  for (int i = 0; i < 16; ++i) O[i] = 0.f;
  bool failed = false;

  const int koff = kvh * (8 * KVREC) + r * 32 + h * 16;
  const int hoff = kvh * (8 * KVREC) + 2240 + r * 4;
  const int voff = kvh * (8 * KVREC) + 1024 + (h * 19 + (r < 18 ? r : 18)) * 16;

#pragma clang loop unroll(disable)
  for (int s = 0; s < 4; ++s) {
    asm volatile("s_waitcnt vmcnt(0)" ::: "memory");
    __builtin_amdgcn_s_barrier();
    asm volatile("" ::: "memory");
    if (s < 3) stage(s + 1);
    const char* sb = lds + (s & 1) * AT_STAGE;
    bf16x8 kA = *(const bf16x8*)(sb + koff);
    unsigned kH = *(const unsigned*)(sb + hoff);
    if (s == 0) {
      f32x16 S0 = qk_tile(kA, kH, qA, qB);
      m = bf16r(tile_max(S0));
      qB[1] = h ? 0u : pack_bf16(-m, 0.f);
    }
    f32x16 S = qk_tile(kA, kH, qA, qB);
#pragma unroll
    for (int i = 0; i < 8; ++i) {
      bf16x8 v0 = *(const bf16x8*)(sb + voff + i * KVREC);
      bf16x8 v1 = *(const bf16x8*)(sb + voff + i * KVREC + 608);
      f32x16 Sn;
      if (i < 7) {
        kA = *(const bf16x8*)(sb + koff + (i + 1) * KVREC);
        kH = *(const unsigned*)(sb + hoff + (i + 1) * KVREC);
        Sn = qk_tile(kA, kH, qA, qB);
      }
      pv_tile(S, v0, v1, O);
      if (i < 7) S = Sn;
    }
    float lv = h ? 0.f : O[10];
    if (__any(!(lv < 4294967296.f))) {
      auto rr = __builtin_amdgcn_permlane32_swap(__float_as_uint(lv), __float_as_uint(lv), false, false);
      lv = __uint_as_float(rr[0]);
      if (__any(!(lv < 3.0e38f))) failed = true;
      float sh = lv > 1.f && lv < 3.0e38f ? floorf(__builtin_amdgcn_logf(lv)) : 0.f;
      float mn = bf16r(m + sh);
      float sc = __builtin_amdgcn_exp2f(m - mn);
      m = mn;
#pragma unroll
      for (int i = 0; i < 16; ++i) O[i] *= sc;
      qB[1] = h ? 0u : pack_bf16(-m, 0.f);
    }
  }

  if (failed) {
    const char* rec = KV + (size_t)(head * 64 + kvh * 32) * KVREC;
    qB[1] = 0u;
    m = -3.0e38f;
#pragma unroll
    for (int i = 0; i < 16; ++i) O[i] = 0.f;
#pragma clang loop unroll(disable)
    for (int it = 0; it < 32; ++it, rec += KVREC) {
      bf16x8 kA = *(const bf16x8*)(rec + r * 32 + h * 16);
      unsigned kH = *(const unsigned*)(rec + 2240 + r * 4);
      bf16x8 v0 = *(const bf16x8*)(rec + 1024 + (h * 19 + (r < 18 ? r : 18)) * 16);
      bf16x8 v1 = *(const bf16x8*)(rec + 1024 + 608 + (h * 19 + (r < 18 ? r : 18)) * 16);
      f32x16 S = qk_tile(kA, kH, qA, qB);
      float mn = fmaxf(m, tile_max(S));
      float sc = __builtin_amdgcn_exp2f(m - mn);
      m = mn;
#pragma unroll
      for (int i = 0; i < 16; ++i) { S[i] -= mn; O[i] *= sc; }
      pv_tile(S, v0, v1, O);
    }
  }

  __syncthreads();
  float* mrg = (float*)lds;
  if (kvh == 1) {
#pragma unroll
    for (int i = 0; i < 12; ++i) mrg[(qt * 13 + i) * 64 + l] = O[i];
    mrg[(qt * 13 + 12) * 64 + l] = m;
  }
  __syncthreads();
  if (kvh == 0) {
    float m1 = mrg[(qt * 13 + 12) * 64 + l];
    float mm = fmaxf(m, m1);
    float a0 = __builtin_amdgcn_exp2f(m - mm), a1 = __builtin_amdgcn_exp2f(m1 - mm);
    float o[12];
#pragma unroll
    for (int i = 0; i < 12; ++i) o[i] = O[i] * a0 + mrg[(qt * 13 + i) * 64 + l] * a1;
    float lsum = __shfl(o[10], r);
    float inv = 1.0f / lsum;
#pragma unroll
    for (int pr = 0; pr < 5; ++pr) {
      if (pr == 4 && h) continue;
      int d = (pr >> 1) * 8 + (pr & 1) * 2 + 4 * h;
      int c = head * 18 + d;
      unsigned v = pack_bf16(o[2 * pr] * inv, o[2 * pr + 1] * inv);
      *(unsigned*)(oT + ((size_t)(c >> 3) * SEQ + token) * 8 + (c & 7)) = v;
    }
  }
}

template <int N> DI void wait_vm() { asm volatile("s_waitcnt vmcnt(%0)" ::"i"(N) : "memory"); }
DI void raw_barrier() {
  __builtin_amdgcn_s_barrier();
  asm volatile("" ::: "memory");
}

template <int NW, int RT>
struct Gemm2 {
  static constexpr int BM = NW * RT * 16;
  static constexpr int AB = 8 * BM * 16;
  static constexpr int STG = AB + 18432;
  static constexpr int NAW = AB / 1024 / NW;
  static constexpr int NWW = (18 + NW - 1) / NW;
  static constexpr int NV = NAW + NWW;
  static constexpr int LA = 4;

  const char* abase;
  const char* wbase;
  unsigned avo[NAW], wvo[NWW];
  unsigned lds_base;
  char* lds;
  int w, aoff, woff0, woff1;
  bf16x8 af[2][RT], wq_[LA];

  DI void init(const u16* AT, const u16* W, int tok0, int col0, int kbeg, char* lds_, int w_, int l) {
    lds = lds_; w = w_;
    lds_base = (unsigned)(size_t)(LDS_AS char*)lds_;
    abase = (const char*)(AT + (size_t)(kbeg / 8) * SEQ * 8);
    wbase = (const char*)(W + (size_t)kbeg * DM);
    const int g = l >> 4;
#pragma unroll
    for (int i = 0; i < NAW; ++i) {
      int off = (w * NAW + i) * 64 + l;
      int chunk = off / BM, token = off - chunk * BM;
      avo[i] = (unsigned)((chunk * SEQ + tok0 + token) * 16);
    }
#pragma unroll
    for (int i = 0; i < NWW; ++i) {
      int n = w + NW * i;
      if (n >= 18) n -= 2;
      int c = n * 64 + l;
      int lr = c / 18, ch = c - lr * 18;
      int krow = lr ^ ((lr & 8) >> 1);
      wvo[i] = (unsigned)((krow * DM + col0 + ch * 8) * 2);
    }
    aoff = g * (BM * 16) + (w * RT * 16 + (l & 15)) * 16;
    const int wq = (l & 15) >> 2, wp = l & 3;
    woff0 = AB + (g * 8 + 4 * (0 ^ (g & 1)) + wq) * 288 + wp * 8;
    woff1 = AB + (g * 8 + 4 * (1 ^ (g & 1)) + wq) * 288 + wp * 8;
  }
  DI void piece(int t, int p) {
    const unsigned sb = lds_base + (t & 3) * STG;
    if (p < NAW) {
      glds16_asm(avo[p], abase + (size_t)t * (8 * SEQ * 16), sb + (w * NAW + p) * 1024);
    } else {
      const int i = p - NAW;
      int n = w + NW * i;
      if (n >= 18) n -= 2;
      glds16_asm(wvo[i], wbase + (size_t)t * (64 * DM * 2), sb + AB + n * 1024);
    }
  }
  DI bf16x8 load_w(const char* sb, int grp) {
    const int kk = grp / 9, ct = grp - kk * 9;
    s16x4 lo = tr_read(sb + woff0 + kk * 9216 + ct * 32);
    s16x4 hi = tr_read(sb + woff1 + kk * 9216 + ct * 32);
    return __builtin_shufflevector(lo, hi, 0, 1, 2, 3, 4, 5, 6, 7);
  }
  DI bf16x8 load_a(const char* sb, int kk, int rt) {
    return *(const bf16x8*)(sb + aoff + kk * 4 * BM * 16 + rt * 256);
  }
  DI void prologue_issue() {
#pragma unroll
    for (int t = 0; t < 3; ++t)
#pragma unroll
      for (int p = 0; p < NV; ++p) piece(t, p);
  }
  DI void prologue_read() {
    wait_vm<2 * NV>();
    raw_barrier();
#pragma unroll
    for (int kk = 0; kk < 2; ++kk)
#pragma unroll
      for (int rt = 0; rt < RT; ++rt) af[kk][rt] = load_a(lds, kk, rt);
#pragma unroll
    for (int i = 0; i < LA; ++i) wq_[i] = load_w(lds, i);
  }
  template <bool ISSUE, bool PREF>
  DI void body(int t, f32x4 (&acc)[9][RT]) {
    const char* sb = lds + (t & 3) * STG;
    const char* sn = lds + ((t + 1) & 3) * STG;
    bf16x8 an[2][RT];
#pragma unroll
    for (int grp = 0; grp < 18; ++grp) {
      bf16x8 wf = wq_[0];
#pragma unroll
      for (int i = 0; i + 1 < LA; ++i) wq_[i] = wq_[i + 1];
      if (grp + LA < 18) wq_[LA - 1] = load_w(sb, grp + LA);
      else if (PREF) wq_[LA - 1] = load_w(sn, grp + LA - 18);
      if (PREF && grp >= 8 && grp < 8 + 2 * RT) an[(grp - 8) / RT][(grp - 8) % RT] = load_a(sn, (grp - 8) / RT, (grp - 8) % RT);
      if (ISSUE && grp < NV) piece(t + 3, grp);
      const int kk = grp / 9, ct = grp - kk * 9;
#pragma unroll
      for (int rt = 0; rt < RT; ++rt) acc[ct][rt] = MFMA16(wf, af[kk][rt], acc[ct][rt]);
      __builtin_amdgcn_sched_barrier(0);
    }
    if (PREF) {
#pragma unroll
      for (int kk = 0; kk < 2; ++kk)
#pragma unroll
        for (int rt = 0; rt < RT; ++rt) af[kk][rt] = an[kk][rt];
    }
  }
  DI void run(int nsteps, f32x4 (&acc)[9][RT]) {
    prologue_read();
    int tl = nsteps - 3;
    asm volatile("" : "+s"(tl));
#pragma clang loop unroll(disable)
    for (int t = 0; t < tl; ++t) {
      wait_vm<NV>();
      raw_barrier();
      body<true, true>(t, acc);
    }
    wait_vm<NV>();
    raw_barrier();
    body<false, true>(tl, acc);
    wait_vm<0>();
    raw_barrier();
    body<false, true>(tl + 1, acc);
    body<false, false>(tl + 2, acc);
  }
};

__global__ __launch_bounds__(256, 1) void down_kernel(const u16* __restrict__ hT, const u16* __restrict__ wd,
                                                      float* __restrict__ part) {
  using G = Gemm2<4, 2>;
  __shared__ __attribute__((aligned(16))) char lds[4 * G::STG];
  const int b = blockIdx.x;
  const int xcd = b & 7, idx = b >> 3;
  const int kn = xcd * 2 + (idx & 1);
  const int bm = idx >> 1;
  const int ks = kn >> 2, bn = kn & 3;
  const int w = __builtin_amdgcn_readfirstlane(threadIdx.x >> 6);
  const int l = threadIdx.x & 63;
  G gm;
  gm.init(hT, wd, bm * 128, bn * 144, ks * 1152, lds, w, l);
  gm.prologue_issue();
  f32x4 acc[9][2];
#pragma unroll
  for (int i = 0; i < 9; ++i) { acc[i][0] = (f32x4){0.f, 0.f, 0.f, 0.f}; acc[i][1] = (f32x4){0.f, 0.f, 0.f, 0.f}; }
  gm.run(18, acc);
#pragma unroll
  for (int ct = 0; ct < 9; ++ct)
#pragma unroll
    for (int rt = 0; rt < 2; ++rt) {
      int tt = bm * 8 + w * 2 + rt, ctg = bn * 9 + ct;
      *(f32x4*)(part + ((size_t)(ks * 128 + tt) * 36 + ctg) * 256 + l * 4) = acc[ct][rt];
    }
}

__global__ __launch_bounds__(128, 1) void final_kernel(const u16* __restrict__ oT, const u16* __restrict__ wo,
                                                       const float* __restrict__ part, const u16* __restrict__ x,
                                                       const u16* __restrict__ gw, u16* __restrict__ out) {
  using G = Gemm2<2, 1>;
  __shared__ __attribute__((aligned(16))) char lds[4 * G::STG + 128];
  const int b = blockIdx.x;
  const int bn = b & 3, bm = b >> 2;
  const int w = __builtin_amdgcn_readfirstlane(threadIdx.x >> 6);
  const int l = threadIdx.x & 63;
  G gm;
  gm.init(oT, wo, bm * 32, bn * 144, 0, lds, w, l);
  gm.prologue_issue();

  const int tt = bm * 2 + w;
  f32x4 bsum[9];
#pragma unroll
  for (int ct = 0; ct < 9; ++ct) {
    int ctg = bn * 9 + ct;
    f32x4 p0 = *(const f32x4*)(part + ((size_t)(0 * 128 + tt) * 36 + ctg) * 256 + l * 4);
    f32x4 p1 = *(const f32x4*)(part + ((size_t)(1 * 128 + tt) * 36 + ctg) * 256 + l * 4);
    f32x4 p2 = *(const f32x4*)(part + ((size_t)(2 * 128 + tt) * 36 + ctg) * 256 + l * 4);
    f32x4 p3 = *(const f32x4*)(part + ((size_t)(3 * 128 + tt) * 36 + ctg) * 256 + l * 4);
    bsum[ct] = (p0 + p1) + (p2 + p3);
  }
  float* gs = (float*)(lds + 4 * G::STG);
  {
    int tk = threadIdx.x >> 2, part4 = threadIdx.x & 3;
    const u16* xr = x + (size_t)(bm * 32 + tk) * DM + part4 * 144;
    const u16* gr = gw + part4 * 144;
    float s = 0.f;
#pragma unroll
    for (int i = 0; i < 144; i += 8) {
      bf16x8 xv = *(const bf16x8*)(xr + i);
      bf16x8 gv = *(const bf16x8*)(gr + i);
#pragma unroll
      for (int e = 0; e < 8; ++e) s += bf16r(bf2f((u16)xv[e]) * bf2f((u16)gv[e]));
    }
    s += __shfl_xor(s, 1);
    s += __shfl_xor(s, 2);
    s = bf16r(s);
    float gg = bf16r(1.0f / (1.0f + __expf(-s)));
    if (part4 == 0) gs[tk] = gg;
  }
#pragma unroll
  for (int ct = 0; ct < 9; ++ct) asm volatile("" : "+v"(bsum[ct]));
  asm volatile("s_waitcnt lgkmcnt(0)" ::: "memory");
  f32x4 acc[9][1];
#pragma unroll
  for (int i = 0; i < 9; ++i) acc[i][0] = (f32x4){0.f, 0.f, 0.f, 0.f};
  gm.run(9, acc);
  const float gg = gs[w * 16 + (l & 15)];
  const float og = bf16r(1.0f - gg);
  const int token = bm * 32 + w * 16 + (l & 15);
#pragma unroll
  for (int ct = 0; ct < 9; ++ct) {
    int ctg = bn * 9 + ct;
    float o[4];
#pragma unroll
    for (int e = 0; e < 4; ++e) o[e] = bf16r(gg * bf16r(acc[ct][0][e])) + bf16r(og * bf16r(bsum[ct][e]));
    u32x2 ov = {pack_bf16(o[0], o[1]), pack_bf16(o[2], o[3])};
    *(u32x2*)(out + (size_t)token * DM + ctg * 16 + (l >> 4) * 4) = ov;
  }
}

extern "C" void kernel_launch(void* const* d_in, const int* in_sizes, int n_in, void* d_out, int out_size,
                              void* d_ws, size_t ws_size, hipStream_t stream) {
  const u16* x = (const u16*)d_in[0];
  const u16* wqkv = (const u16*)d_in[1];
  const u16* wo = (const u16*)d_in[2];
  const u16* wg = (const u16*)d_in[4];
  const u16* wu = (const u16*)d_in[5];
  const u16* wd = (const u16*)d_in[6];
  const u16* gw = (const u16*)d_in[7];
  char* ws = (char*)d_ws;
  u16* hT = (u16*)(ws + WS_HT);
  u16* Qb = (u16*)(ws + WS_QB);
  char* KV = ws + WS_KV;
  u16* oT = (u16*)(ws + WS_OT);
  float* part = (float*)(ws + WS_PART);
  u16* out = (u16*)d_out;

  gemm1_kernel<<<256, 512, 0, stream>>>(x, wg, wu, wqkv, hT, Qb, KV);
  attn_kernel<<<512, 512, 0, stream>>>(Qb, KV, oT);
  down_kernel<<<256, 256, 0, stream>>>(hT, wd, part);
  final_kernel<<<256, 128, 0, stream>>>(oT, wo, part, x, gw, out);
}
```

```cpp
#include <hip/hip_runtime.h>
#include <hip/hip_bf16.h>
#include <stdint.h>
#include <type_traits>

typedef unsigned short u16;
typedef __attribute__((ext_vector_type(8))) short bf16x8;
typedef __attribute__((ext_vector_type(4))) short s16x4;
typedef __attribute__((ext_vector_type(4))) float f32x4;
typedef __attribute__((ext_vector_type(16))) float f32x16;
typedef __attribute__((ext_vector_type(2))) float f32x2;
typedef __attribute__((ext_vector_type(2))) __bf16 bf16x2_t;
typedef __attribute__((ext_vector_type(2))) unsigned u32x2;
typedef __attribute__((ext_vector_type(4))) unsigned u32x4;

#define DI __device__ __forceinline__
#define LDS_AS __attribute__((address_space(3)))

constexpr int SEQ = 2048, DM = 576, FF = 4608, NH = 32, DH = 18, NQKV = 1728;

DI unsigned pack_bf16(float lo, float hi) {
  f32x2 f = {lo, hi};
  bf16x2_t b = __builtin_convertvector(f, bf16x2_t);
  return __builtin_bit_cast(unsigned, b);
}
DI float bf16r(float x) { return __uint_as_float(pack_bf16(x, x) << 16); }
DI float bf2f(u16 v) { return __uint_as_float(((unsigned)v) << 16); }

DI s16x4 tr_read(const char* lds_ptr) {
  return __builtin_amdgcn_ds_read_tr16_b64_v4i16((LDS_AS s16x4*)(lds_ptr));
}
DI void glds16(const void* gsrc, char* lds_dst) {
  __builtin_amdgcn_global_load_lds((const __attribute__((address_space(1))) void*)gsrc,
                                   (LDS_AS void*)lds_dst, 16, 0, 0);
}
DI int lane_id_opaque() {
  int l;
  asm volatile("v_mbcnt_lo_u32_b32 %0, -1, 0\n\tv_mbcnt_hi_u32_b32 %0, -1, %0" : "=v"(l));
  return l;
}
DI void glds16_asm(unsigned voff, const void* sbase, unsigned lds_dst) {
  unsigned keep;
  asm volatile("s_mov_b32 %0, m0\n\ts_mov_b32 m0, %3\n\ts_nop 0\n\tglobal_load_lds_dwordx4 %1, %2\n\ts_mov_b32 m0, %0"
               : "=&s"(keep) : "v"(voff), "s"(sbase), "s"(lds_dst) : "memory");
}
#define STAMP(kid, i) do {} while (0)
#define MFMA16(a, b, c) __builtin_amdgcn_mfma_f32_16x16x32_bf16((a), (b), (c), 0, 0, 0)
#define MFMA32(a, b, c) __builtin_amdgcn_mfma_f32_32x32x16_bf16((a), (b), (c), 0, 0, 0)

constexpr size_t WS_HT = 0;
constexpr size_t WS_QB = WS_HT + (size_t)FF * SEQ * 2;
constexpr int KVREC = 2432;
constexpr size_t WS_KV = WS_QB + (size_t)NH * SEQ * 32 * 2;
constexpr size_t WS_VF = WS_KV;
constexpr size_t WS_OT = WS_KV + (size_t)NH * 64 * KVREC;
constexpr size_t WS_PART = WS_OT + (size_t)DM * SEQ * 2;
constexpr size_t WS_G = WS_PART + (size_t)4 * SEQ * DM * 4;
constexpr size_t WS_END = WS_G + SEQ * 4;

constexpr int G1_XB = 256 * 64;
constexpr int G1_WB = 9216 * 2 + 5120;
constexpr int G1_STAGE = G1_XB + G1_WB;

template <bool ISV, int WN>
DI void gemm1_body(const u16* __restrict__ x, const u16* __restrict__ wg, const u16* __restrict__ wu,
                   const u16* __restrict__ wqkv, u16* __restrict__ hT, u16* __restrict__ Qb,
                   char* __restrict__ KV, const u16* __restrict__ gw, float* __restrict__ gout, char* lds,
                   int bm, int j, int w) {
  const int l = lane_id_opaque();
  const int wm = w >> 1;
  const int g = l >> 4;
  const int jq = j < 27 ? j : 26;

  unsigned xvo, guvo, evo;
  const char* ebase;
  int estep, edst;
  {
    int row = 32 * w + (l >> 2);
    int chunk = (l & 3) ^ ((-(l >> 4)) & 3);
    xvo = (unsigned)((bm * 256 + row) * DM + chunk * 8) * 2u;
    {
      int c = w * 64 + l;
      int lr = c / 18, ch = c - lr * 18;
      int krow = lr ^ ((lr & 8) >> 1);
      guvo = (unsigned)(krow * FF + j * 144 + ch * 8) * 2u;
    }
    if (w < 2) {
      int c = 8 * 64 + l;
      int lr = c / 18, ch = c - lr * 18;
      int krow = lr ^ ((lr & 8) >> 1);
      evo = (unsigned)(krow * FF + j * 144 + ch * 8) * 2u;
      ebase = (const char*)(w ? wu : wg);
      estep = 32 * FF * 2;
      edst = G1_XB + w * 9216 + 8 * 1024;
    } else {
      int n = w - 2;
      if (n > 4) n = 4;
      int c = n * 64 + l;
      int lr = c / 10, ch = c - lr * 10;
      if (ch > 7) ch = 7;
      int krow = lr ^ ((lr & 8) >> 1);
      evo = (unsigned)(krow * NQKV + jq * 64 + ch * 8) * 2u;
      ebase = (const char*)wqkv;
      estep = 32 * NQKV * 2;
      edst = G1_XB + 18432 + n * 1024;
    }
  }

  const unsigned lds_base = (unsigned)(size_t)(LDS_AS char*)lds;
  auto stage_piece = [&](int t, int p) {
    const unsigned sb = lds_base + (t & 3) * G1_STAGE;
    if (p < 2) {
      const char* xb = (const char*)x + t * 64 + p * 16 * DM * 2;
      glds16_asm(xvo, xb, sb + (32 * w + 16 * p) * 64);
    } else if (p < 4) {
      const char* gb = (const char*)((p & 1) ? wu : wg) + (size_t)t * (32 * FF * 2);
      glds16_asm(guvo, gb, sb + G1_XB + (p & 1) * 9216 + w * 1024);
    } else {
      glds16_asm(evo, ebase + (size_t)t * estep, sb + edst);
    }
  };

  f32x4 acc[11][4];
#pragma unroll
  for (int i = 0; i < 11; ++i)
#pragma unroll
    for (int r = 0; r < 4; ++r) acc[i][r] = (f32x4){0.f, 0.f, 0.f, 0.f};

  const int xoff = wm * 4096 + (l & 15) * 64 + ((g ^ ((-((l & 15) >> 2)) & 3)) << 4);
  const int wq = (l & 15) >> 2, wp = l & 3;
  const int lr0 = g * 8 + 4 * (0 ^ (g & 1)) + wq, lr1 = g * 8 + 4 * (1 ^ (g & 1)) + wq;
  const int woff0 = G1_XB + lr0 * 288 + wp * 8, woff1 = G1_XB + lr1 * 288 + wp * 8;
  const int qoff0 = G1_XB + 18432 + lr0 * 160 + wp * 8, qoff1 = G1_XB + 18432 + lr1 * 160 + wp * 8;

  auto load_w = [&](const char* sb, int i) -> bf16x8 {
    const bool isq = (i == 10) || (WN == 1 && (i == 4 || i == 9));
    s16x4 lo, hi;
    if (isq) {
      const int qct = WN == 0 ? 0 : (i == 4 ? 1 : (i == 9 ? 2 : 3));
      lo = tr_read(sb + qoff0 + qct * 32);
      hi = tr_read(sb + qoff1 + qct * 32);
    } else {
      const int off = (i < 5 ? (WN * 5 + i) * 32 : 9216 + (WN * 5 + i - 5) * 32);
      lo = tr_read(sb + woff0 + off);
      hi = tr_read(sb + woff1 + off);
    }
    return __builtin_shufflevector(lo, hi, 0, 1, 2, 3, 4, 5, 6, 7);
  };

  bf16x8 xf[4], wfa, wfb;
  auto body = [&](int t, auto issue_c, auto pref_c) {
    constexpr bool ISSUE = decltype(issue_c)::value, PREF = decltype(pref_c)::value;
    const char* sb = lds + (t & 3) * G1_STAGE;
    const char* sn = lds + ((t + 1) & 3) * G1_STAGE;
    bf16x8 xn[4];
#pragma unroll
    for (int i = 0; i < 11; ++i) {
      bf16x8 wf = wfa;
      wfa = wfb;
      if (i + 2 < 11) wfb = load_w(sb, i + 2);
      else if (PREF) wfb = load_w(sn, i + 2 - 11);
      if (PREF && i >= 5 && i < 9) xn[i - 5] = *(const bf16x8*)(sn + xoff + (i - 5) * 1024);
      if (ISSUE && (i & 1) == 0 && i < 10) stage_piece(t + 3, i >> 1);
      const bool isq = (i == 10) || (WN == 1 && (i == 4 || i == 9));
#pragma unroll
      for (int rt = 0; rt < 4; ++rt) {
        if (ISV && isq) acc[i][rt] = MFMA16(xf[rt], wf, acc[i][rt]);
        else acc[i][rt] = MFMA16(wf, xf[rt], acc[i][rt]);
      }
      __builtin_amdgcn_sched_barrier(0);
    }
    if (PREF) {
#pragma unroll
      for (int rt = 0; rt < 4; ++rt) xf[rt] = xn[rt];
    }
  };
  using T_ = std::integral_constant<bool, true>;
  using F_ = std::integral_constant<bool, false>;

  STAMP(0, 0);
#pragma unroll
  for (int t = 0; t < 3; ++t)
#pragma unroll
    for (int p = 0; p < 5; ++p) stage_piece(t, p);
  asm volatile("s_waitcnt vmcnt(10)" ::: "memory");
  __builtin_amdgcn_s_barrier();
  asm volatile("" ::: "memory");
#pragma unroll
  for (int rt = 0; rt < 4; ++rt) xf[rt] = *(const bf16x8*)(lds + xoff + rt * 1024);
  wfa = load_w(lds, 0);
  wfb = load_w(lds, 1);
  int tl = 15;
  asm volatile("" : "+s"(tl));
#pragma clang loop unroll(disable)
  for (int t = 0; t < tl; ++t) {
    asm volatile("s_waitcnt vmcnt(5)" ::: "memory");
    __builtin_amdgcn_s_barrier();
    asm volatile("" ::: "memory");
    if ((t & 1) == 0) STAMP(0, 1 + (t >> 1));
    body(t, T_{}, T_{});
  }
  asm volatile("s_waitcnt vmcnt(5)" ::: "memory");
  __builtin_amdgcn_s_barrier();
  asm volatile("" ::: "memory");
  body(tl, F_{}, T_{});
  asm volatile("s_waitcnt vmcnt(0)" ::: "memory");
  __builtin_amdgcn_s_barrier();
  asm volatile("" ::: "memory");
  STAMP(0, 9);
  body(tl + 1, F_{}, T_{});
  body(tl + 2, F_{}, F_{});

  STAMP(0, 10);
  __builtin_amdgcn_sched_barrier(0);
  {
  const int l = lane_id_opaque();
  const int g = l >> 4;
  const int tok0 = bm * 256 + wm * 64;
  constexpr int NP = WN == 0 ? 5 : 4;
#pragma unroll
  for (int i = 0; i < NP; ++i) {
    const int ct = WN * 5 + i;
    const int chunk = j * 18 + ct * 2 + (g >> 1);
#pragma unroll
    for (int rt = 0; rt < 4; ++rt) {
      float hv[4];
#pragma unroll
      for (int r = 0; r < 4; ++r) hv[r] = bf16r(acc[i][rt][r]) * bf16r(acc[i + 5][rt][r]);
      u32x2 o = {pack_bf16(hv[0], hv[1]), pack_bf16(hv[2], hv[3])};
      int token = tok0 + rt * 16 + (l & 15);
      *(u32x2*)(hT + ((size_t)chunk * SEQ + token) * 8 + (g & 1) * 4) = o;
    }
  }
  if (j >= 28) {
    const int tid = w * 64 + l;
    const int token = bm * 256 + (j - 28) * 64 + (tid >> 3), prt = tid & 7;
    const u16* xr = x + (size_t)token * DM + prt * 72;
    const u16* gr = gw + prt * 72;
    bf16x8 xv[9], gv[9];
#pragma unroll
    for (int i = 0; i < 9; ++i) { xv[i] = *(const bf16x8*)(xr + i * 8); gv[i] = *(const bf16x8*)(gr + i * 8); }
    float sum = 0.f;
#pragma unroll
    for (int i = 0; i < 9; ++i)
#pragma unroll
      for (int e = 0; e < 8; ++e) sum += bf16r(bf2f((u16)xv[i][e]) * bf2f((u16)gv[i][e]));
    sum += __shfl_xor(sum, 1);
    sum += __shfl_xor(sum, 2);
    sum += __shfl_xor(sum, 4);
    sum = bf16r(sum);
    if (prt == 0) gout[token] = bf16r(1.0f / (1.0f + __expf(-sum)));
  }
  if (j < 27) {
    constexpr int NQ = WN == 0 ? 1 : 3;
#pragma unroll
    for (int qi = 0; qi < NQ; ++qi) {
      const int ai = WN == 0 ? 10 : (qi == 0 ? 4 : (qi == 1 ? 9 : 10));
      const int qct = WN == 0 ? 0 : qi + 1;
      if (ISV) {
        int cv = j * 64 + qct * 16 + (l & 15) - 1152;
        int head = cv / 18, d = cv - head * 18;
#pragma unroll
        for (int rt = 0; rt < 4; ++rt) {
          int t16 = tok0 + rt * 16;
          int tile32 = t16 >> 5, s = rt & 1;
          char* dst = KV + (size_t)(head * 64 + tile32) * KVREC + 1024 + ((s * 2 + (g & 1)) * 19 + d) * 16 + (g >> 1) * 8;
          u32x2 o = {pack_bf16(acc[ai][rt][0], acc[ai][rt][1]), pack_bf16(acc[ai][rt][2], acc[ai][rt][3])};
          *(u32x2*)dst = o;
          if (d == 17) *(u32x2*)(dst + 16) = (u32x2){0x3F803F80u, 0x3F803F80u};
        }
      } else {
        const bool isQ = j < 9;
        const float sc = isQ ? 0.23570226039551584f * 1.4426950408889634f : 1.0f;
#pragma unroll
        for (int pr = 0; pr < 2; ++pr) {
          int c = j * 64 + qct * 16 + 4 * g + 2 * pr - (isQ ? 0 : 576);
          int head = c / 18, d = c - head * 18;
#pragma unroll
          for (int rt = 0; rt < 4; ++rt) {
            int token = tok0 + rt * 16 + (l & 15);
            unsigned v = pack_bf16(acc[ai][rt][2 * pr] * sc, acc[ai][rt][2 * pr + 1] * sc);
            char* dst;
            if (isQ) dst = (char*)(Qb + ((size_t)head * SEQ + token) * 32 + d);
            else {
              char* rec = KV + (size_t)(head * 64 + (token >> 5)) * KVREC;
              dst = d < 16 ? rec + (token & 31) * 32 + d * 2 : rec + 2240 + (token & 31) * 4;
            }
            *(unsigned*)dst = v;
          }
        }
      }
    }
  }
  }
  STAMP(0, 11);
}

__global__ __launch_bounds__(512, 2) void gemm1_kernel(const u16* __restrict__ x, const u16* __restrict__ wg,
                                                       const u16* __restrict__ wu, const u16* __restrict__ wqkv,
                                                       u16* __restrict__ hT, u16* __restrict__ Qb,
                                                       char* __restrict__ KV, const u16* __restrict__ gw,
                                                       float* __restrict__ gout) {
  __shared__ __attribute__((aligned(16))) char lds[4 * G1_STAGE];
  const int b = blockIdx.x;
  const int xcd = b & 7, idx = b >> 3;
  const int j = xcd * 4 + (idx & 3), bm = idx >> 2;
  const int w = __builtin_amdgcn_readfirstlane(threadIdx.x >> 6);
  const bool isv = (j >= 18);
  if (w & 1) {
    if (isv) gemm1_body<true, 1>(x, wg, wu, wqkv, hT, Qb, KV, gw, gout, lds, bm, j, w);
    else gemm1_body<false, 1>(x, wg, wu, wqkv, hT, Qb, KV, gw, gout, lds, bm, j, w);
  } else {
    if (isv) gemm1_body<true, 0>(x, wg, wu, wqkv, hT, Qb, KV, gw, gout, lds, bm, j, w);
    else gemm1_body<false, 0>(x, wg, wu, wqkv, hT, Qb, KV, gw, gout, lds, bm, j, w);
  }
}

DI float xhalf_max(float v) {
  auto rr = __builtin_amdgcn_permlane32_swap(__float_as_uint(v), __float_as_uint(v), false, false);
  return fmaxf(__uint_as_float(rr[0]), __uint_as_float(rr[1]));
}
DI float tile_max(const f32x16& S) {
  float t = fmaxf(fmaxf(S[0], S[1]), S[2]);
#pragma unroll
  for (int i = 3; i < 15; i += 2) t = fmaxf(fmaxf(t, S[i]), S[i + 1]);
  return xhalf_max(fmaxf(t, S[15]));
}
DI f32x16 qk_tile(bf16x8 kA, unsigned kH, bf16x8 qA, u32x4 qB) {
  f32x16 S;
#pragma unroll
  for (int i = 0; i < 16; ++i) S[i] = 0.f;
  u32x4 kBv = {kH, 0x00003F80u, 0u, 0u};
  S = MFMA32(kA, qA, S);
  S = MFMA32(__builtin_bit_cast(bf16x8, kBv), __builtin_bit_cast(bf16x8, qB), S);
  return S;
}
DI void pv_tile(f32x16& S, bf16x8 v0, bf16x8 v1, f32x16& O) {
#pragma unroll
  for (int i = 0; i < 16; ++i) S[i] = __builtin_amdgcn_exp2f(S[i]);
  u32x4 p0 = {pack_bf16(S[0], S[1]), pack_bf16(S[2], S[3]), pack_bf16(S[4], S[5]), pack_bf16(S[6], S[7])};
  u32x4 p1 = {pack_bf16(S[8], S[9]), pack_bf16(S[10], S[11]), pack_bf16(S[12], S[13]), pack_bf16(S[14], S[15])};
  O = MFMA32(v0, __builtin_bit_cast(bf16x8, p0), O);
  O = MFMA32(v1, __builtin_bit_cast(bf16x8, p1), O);
}

constexpr int AT_STAGE = 2 * 8 * KVREC;

__global__ __launch_bounds__(512, 4) void attn_kernel(const u16* __restrict__ Qb, const char* __restrict__ KV,
                                                      u16* __restrict__ oT) {
  __shared__ __attribute__((aligned(16))) char lds[2 * AT_STAGE];
  const int b = blockIdx.x;
  const int head = (b & 7) * 4 + ((b >> 3) & 3), qblk = b >> 5;
  const int w = __builtin_amdgcn_readfirstlane(threadIdx.x >> 6);
  const int l = threadIdx.x & 63, r = l & 31, h = l >> 5;
  const int qt = w & 3, kvh = w >> 2;
  const int token = qblk * 128 + qt * 32 + r;

  const unsigned lds_base = (unsigned)(size_t)(LDS_AS char*)lds;
  const char* kvsrc = KV + (size_t)(head * 64) * KVREC;
  auto stage = [&](int s) {
#pragma unroll
    for (int i = 0; i < 5; ++i) {
      int n = w * 5 + i;
      if (n > 37) n -= 2;
      const int hf = n >= 19 ? 1 : 0, pc = n - hf * 19;
      glds16_asm((unsigned)(l * 16), kvsrc + (hf * 32 + s * 8) * KVREC + pc * 1024,
                 lds_base + (s & 1) * AT_STAGE + hf * (8 * KVREC) + pc * 1024);
    }
  };
  STAMP(1, 0);
  stage(0);

  const u16* qrow = Qb + ((size_t)head * SEQ + token) * 32;
  bf16x8 qA = *(const bf16x8*)(qrow + 8 * h);
  unsigned q67 = *(const unsigned*)(qrow + 16);
  asm volatile("" : "+v"(qA), "+v"(q67));
  u32x4 qB = {h ? 0u : q67, 0u, 0u, 0u};
  float m = 0.f;
  f32x16 O;
#pragma unroll
  for (int i = 0; i < 16; ++i) O[i] = 0.f;
  bool failed = false;

  const int koff = kvh * (8 * KVREC) + r * 32 + h * 16;
  const int hoff = kvh * (8 * KVREC) + 2240 + r * 4;
  const int voff = kvh * (8 * KVREC) + 1024 + (h * 19 + (r < 18 ? r : 18)) * 16;

#pragma clang loop unroll(disable)
  for (int s = 0; s < 4; ++s) {
    asm volatile("s_waitcnt vmcnt(0)" ::: "memory");
    __builtin_amdgcn_s_barrier();
    asm volatile("" ::: "memory");
    STAMP(1, 1 + s);
    if (s < 3) stage(s + 1);
    const char* sb = lds + (s & 1) * AT_STAGE;
    bf16x8 kA = *(const bf16x8*)(sb + koff);
    unsigned kH = *(const unsigned*)(sb + hoff);
    if (s == 0) {
      f32x16 S0 = qk_tile(kA, kH, qA, qB);
      m = bf16r(tile_max(S0));
      qB[1] = h ? 0u : pack_bf16(-m, 0.f);
    }
    f32x16 S = qk_tile(kA, kH, qA, qB);
#pragma unroll
    for (int i = 0; i < 8; ++i) {
      bf16x8 v0 = *(const bf16x8*)(sb + voff + i * KVREC);
      bf16x8 v1 = *(const bf16x8*)(sb + voff + i * KVREC + 608);
      f32x16 Sn;
      if (i < 7) {
        kA = *(const bf16x8*)(sb + koff + (i + 1) * KVREC);
        kH = *(const unsigned*)(sb + hoff + (i + 1) * KVREC);
        Sn = qk_tile(kA, kH, qA, qB);
      }
      pv_tile(S, v0, v1, O);
      if (i < 7) S = Sn;
    }
    float lv = h ? 0.f : O[10];
    if (__any(!(lv < 4294967296.f))) {
      auto rr = __builtin_amdgcn_permlane32_swap(__float_as_uint(lv), __float_as_uint(lv), false, false);
      lv = __uint_as_float(rr[0]);
      if (__any(!(lv < 3.0e38f))) failed = true;
      float sh = lv > 1.f && lv < 3.0e38f ? floorf(__builtin_amdgcn_logf(lv)) : 0.f;
      float mn = bf16r(m + sh);
      float sc = __builtin_amdgcn_exp2f(m - mn);
      m = mn;
#pragma unroll
      for (int i = 0; i < 16; ++i) O[i] *= sc;
      qB[1] = h ? 0u : pack_bf16(-m, 0.f);
    }
  }

  STAMP(1, 5);
  if (failed) {
    const char* rec = KV + (size_t)(head * 64 + kvh * 32) * KVREC;
    qB[1] = 0u;
    m = -3.0e38f;
#pragma unroll
    for (int i = 0; i < 16; ++i) O[i] = 0.f;
#pragma clang loop unroll(disable)
    for (int it = 0; it < 32; ++it, rec += KVREC) {
      bf16x8 kA = *(const bf16x8*)(rec + r * 32 + h * 16);
      unsigned kH = *(const unsigned*)(rec + 2240 + r * 4);
      bf16x8 v0 = *(const bf16x8*)(rec + 1024 + (h * 19 + (r < 18 ? r : 18)) * 16);
      bf16x8 v1 = *(const bf16x8*)(rec + 1024 + 608 + (h * 19 + (r < 18 ? r : 18)) * 16);
      f32x16 S = qk_tile(kA, kH, qA, qB);
      float mn = fmaxf(m, tile_max(S));
      float sc = __builtin_amdgcn_exp2f(m - mn);
      m = mn;
#pragma unroll
      for (int i = 0; i < 16; ++i) { S[i] -= mn; O[i] *= sc; }
      pv_tile(S, v0, v1, O);
    }
  }

  __syncthreads();
  float* mrg = (float*)lds;
  if (kvh == 1) {
#pragma unroll
    for (int i = 0; i < 12; ++i) mrg[(qt * 13 + i) * 64 + l] = O[i];
    mrg[(qt * 13 + 12) * 64 + l] = m;
  }
  __syncthreads();
  if (kvh == 0) {
    float m1 = mrg[(qt * 13 + 12) * 64 + l];
    float mm = fmaxf(m, m1);
    float a0 = __builtin_amdgcn_exp2f(m - mm), a1 = __builtin_amdgcn_exp2f(m1 - mm);
    float o[12];
#pragma unroll
    for (int i = 0; i < 12; ++i) o[i] = O[i] * a0 + mrg[(qt * 13 + i) * 64 + l] * a1;
    float lsum = __shfl(o[10], r);
    float inv = 1.0f / lsum;
#pragma unroll
    for (int pr = 0; pr < 5; ++pr) {
      if (pr == 4 && h) continue;
      int d = (pr >> 1) * 8 + (pr & 1) * 2 + 4 * h;
      int c = head * 18 + d;
      unsigned v = pack_bf16(o[2 * pr] * inv, o[2 * pr + 1] * inv);
      *(unsigned*)(oT + ((size_t)(c >> 3) * SEQ + token) * 8 + (c & 7)) = v;
    }
  }
  STAMP(1, 6);
}

template <int N> DI void wait_vm() { asm volatile("s_waitcnt vmcnt(%0)" ::"i"(N) : "memory"); }
DI void raw_barrier() {
  __builtin_amdgcn_s_barrier();
  asm volatile("" ::: "memory");
}

template <int NW, int RT>
struct Gemm2 {
  static constexpr int BM = NW * RT * 16;
  static constexpr int AB = 8 * BM * 16;
  static constexpr int STG = AB + 18432;
  static constexpr int NAW = AB / 1024 / NW;
  static constexpr int NWW = (18 + NW - 1) / NW;
  static constexpr int NV = NAW + NWW;
  static constexpr int LA = 4;

  const char* abase;
  const char* wbase;
  unsigned avo[NAW], wvo[NWW];
  unsigned lds_base;
  char* lds;
  int w, aoff, woff0, woff1;
  bf16x8 af[2][RT], wq_[LA];

  DI void init(const u16* AT, const u16* W, int tok0, int col0, int kbeg, char* lds_, int w_, int l) {
    lds = lds_; w = w_;
    lds_base = (unsigned)(size_t)(LDS_AS char*)lds_;
    abase = (const char*)(AT + (size_t)(kbeg / 8) * SEQ * 8);
    wbase = (const char*)(W + (size_t)kbeg * DM);
    const int g = l >> 4;
#pragma unroll
    for (int i = 0; i < NAW; ++i) {
      int off = (w * NAW + i) * 64 + l;
      int chunk = off / BM, token = off - chunk * BM;
      avo[i] = (unsigned)((chunk * SEQ + tok0 + token) * 16);
    }
#pragma unroll
    for (int i = 0; i < NWW; ++i) {
      int n = w + NW * i;
      if (n >= 18) n -= 2;
      int c = n * 64 + l;
      int lr = c / 18, ch = c - lr * 18;
      int krow = lr ^ ((lr & 8) >> 1);
      wvo[i] = (unsigned)((krow * DM + col0 + ch * 8) * 2);
    }
    aoff = g * (BM * 16) + (w * RT * 16 + (l & 15)) * 16;
    const int wq = (l & 15) >> 2, wp = l & 3;
    woff0 = AB + (g * 8 + 4 * (0 ^ (g & 1)) + wq) * 288 + wp * 8;
    woff1 = AB + (g * 8 + 4 * (1 ^ (g & 1)) + wq) * 288 + wp * 8;
  }
  DI void piece(int t, int p) {
    const unsigned sb = lds_base + (t & 3) * STG;
    if (p < NAW) {
      glds16_asm(avo[p], abase + (size_t)t * (8 * SEQ * 16), sb + (w * NAW + p) * 1024);
    } else {
      const int i = p - NAW;
      int n = w + NW * i;
      if (n >= 18) n -= 2;
      glds16_asm(wvo[i], wbase + (size_t)t * (64 * DM * 2), sb + AB + n * 1024);
    }
  }
  DI bf16x8 load_w(const char* sb, int grp) {
    const int kk = grp / 9, ct = grp - kk * 9;
    s16x4 lo = tr_read(sb + woff0 + kk * 9216 + ct * 32);
    s16x4 hi = tr_read(sb + woff1 + kk * 9216 + ct * 32);
    return __builtin_shufflevector(lo, hi, 0, 1, 2, 3, 4, 5, 6, 7);
  }
  DI bf16x8 load_a(const char* sb, int kk, int rt) {
    return *(const bf16x8*)(sb + aoff + kk * 4 * BM * 16 + rt * 256);
  }
  DI void prologue_issue() {
#pragma unroll
    for (int t = 0; t < 3; ++t)
#pragma unroll
      for (int p = 0; p < NV; ++p) piece(t, p);
  }
  DI void prologue_read() {
    wait_vm<2 * NV>();
    raw_barrier();
#pragma unroll
    for (int kk = 0; kk < 2; ++kk)
#pragma unroll
      for (int rt = 0; rt < RT; ++rt) af[kk][rt] = load_a(lds, kk, rt);
#pragma unroll
    for (int i = 0; i < LA; ++i) wq_[i] = load_w(lds, i);
  }
  template <bool ISSUE, bool PREF>
  DI void body(int t, f32x4 (&acc)[9][RT]) {
    const char* sb = lds + (t & 3) * STG;
    const char* sn = lds + ((t + 1) & 3) * STG;
    bf16x8 an[2][RT];
#pragma unroll
    for (int grp = 0; grp < 18; ++grp) {
      bf16x8 wf = wq_[0];
#pragma unroll
      for (int i = 0; i + 1 < LA; ++i) wq_[i] = wq_[i + 1];
      if (grp + LA < 18) wq_[LA - 1] = load_w(sb, grp + LA);
      else if (PREF) wq_[LA - 1] = load_w(sn, grp + LA - 18);
      if (PREF && grp >= 8 && grp < 8 + 2 * RT) an[(grp - 8) / RT][(grp - 8) % RT] = load_a(sn, (grp - 8) / RT, (grp - 8) % RT);
      if (ISSUE && grp < NV) piece(t + 3, grp);
      const int kk = grp / 9, ct = grp - kk * 9;
#pragma unroll
      for (int rt = 0; rt < RT; ++rt) acc[ct][rt] = MFMA16(wf, af[kk][rt], acc[ct][rt]);
      __builtin_amdgcn_sched_barrier(0);
    }
    if (PREF) {
#pragma unroll
      for (int kk = 0; kk < 2; ++kk)
#pragma unroll
        for (int rt = 0; rt < RT; ++rt) af[kk][rt] = an[kk][rt];
    }
  }
  DI void run(int nsteps, f32x4 (&acc)[9][RT]) {
    prologue_read();
    int tl = nsteps - 3;
    asm volatile("" : "+s"(tl));
#pragma clang loop unroll(disable)
    for (int t = 0; t < tl; ++t) {
      wait_vm<NV>();
      raw_barrier();
      body<true, true>(t, acc);
    }
    wait_vm<NV>();
    raw_barrier();
    body<false, true>(tl, acc);
    wait_vm<0>();
    raw_barrier();
    body<false, true>(tl + 1, acc);
    body<false, false>(tl + 2, acc);
  }
};

__global__ __launch_bounds__(256, 1) void down_kernel(const u16* __restrict__ hT, const u16* __restrict__ wd,
                                                      u16* __restrict__ part) {
  using G = Gemm2<4, 2>;
  __shared__ __attribute__((aligned(16))) char lds[4 * G::STG];
  const int b = blockIdx.x;
  const int xcd = b & 7, idx = b >> 3;
  const int ks = xcd >> 1;
  const int bm = (xcd & 1) * 8 + (idx >> 2);
  const int bn = idx & 3;
  const int w = __builtin_amdgcn_readfirstlane(threadIdx.x >> 6);
  const int l = threadIdx.x & 63;
  STAMP(2, 0);
  G gm;
  gm.init(hT, wd, bm * 128, bn * 144, ks * 1152, lds, w, l);
  gm.prologue_issue();
  STAMP(2, 1);
  f32x4 acc[9][2];
#pragma unroll
  for (int i = 0; i < 9; ++i) { acc[i][0] = (f32x4){0.f, 0.f, 0.f, 0.f}; acc[i][1] = (f32x4){0.f, 0.f, 0.f, 0.f}; }
  gm.run(18, acc);
  STAMP(2, 2);
#pragma unroll
  for (int ct = 0; ct < 9; ++ct)
#pragma unroll
    for (int rt = 0; rt < 2; ++rt) {
      int tt = bm * 8 + w * 2 + rt, ctg = bn * 9 + ct;
      u32x2 pv = {pack_bf16(acc[ct][rt][0], acc[ct][rt][1]), pack_bf16(acc[ct][rt][2], acc[ct][rt][3])};
      *(u32x2*)(part + ((size_t)(ks * 128 + tt) * 36 + ctg) * 256 + l * 4) = pv;
    }
  STAMP(2, 3);
}

__global__ __launch_bounds__(256, 1) void final_kernel(const u16* __restrict__ oT, const u16* __restrict__ wo,
                                                       const u16* __restrict__ part, const float* __restrict__ gin,
                                                       u16* __restrict__ out) {
  using G = Gemm2<4, 1>;
  __shared__ __attribute__((aligned(16))) char lds[4 * G::STG];
  const int b = blockIdx.x;
  const int bn = b & 3, bm = b >> 2;
  const int w = __builtin_amdgcn_readfirstlane(threadIdx.x >> 6);
  const int l = threadIdx.x & 63;
  STAMP(3, 0);
  G gm;
  gm.init(oT, wo, bm * 64, bn * 144, 0, lds, w, l);
  gm.prologue_issue();
  STAMP(3, 1);

  const int tt = bm * 4 + w;
  const int token = bm * 64 + w * 16 + (l & 15);
  float gg = gin[token];
  f32x4 bsum[9];
#pragma unroll
  for (int ct = 0; ct < 9; ++ct) {
    int ctg = bn * 9 + ct;
    f32x4 pp[4];
#pragma unroll
    for (int ks = 0; ks < 4; ++ks) {
      u32x2 v = *(const u32x2*)(part + ((size_t)(ks * 128 + tt) * 36 + ctg) * 256 + l * 4);
      pp[ks] = (f32x4){__uint_as_float(v[0] << 16), __uint_as_float(v[0] & 0xffff0000u),
                       __uint_as_float(v[1] << 16), __uint_as_float(v[1] & 0xffff0000u)};
    }
    bsum[ct] = (pp[0] + pp[1]) + (pp[2] + pp[3]);
  }
#pragma unroll
  for (int ct = 0; ct < 9; ++ct) asm volatile("" : "+v"(bsum[ct]));
  asm volatile("" : "+v"(gg));
  STAMP(3, 2);
  f32x4 acc[9][1];
#pragma unroll
  for (int i = 0; i < 9; ++i) acc[i][0] = (f32x4){0.f, 0.f, 0.f, 0.f};
  gm.run(9, acc);
  STAMP(3, 3);
  const float og = bf16r(1.0f - gg);
#pragma unroll
  for (int ct = 0; ct < 9; ++ct) {
    int ctg = bn * 9 + ct;
    float o[4];
#pragma unroll
    for (int e = 0; e < 4; ++e) o[e] = bf16r(gg * bf16r(acc[ct][0][e])) + bf16r(og * bf16r(bsum[ct][e]));
    u32x2 ov = {pack_bf16(o[0], o[1]), pack_bf16(o[2], o[3])};
    *(u32x2*)(out + (size_t)token * DM + ctg * 16 + (l >> 4) * 4) = ov;
  }
  STAMP(3, 4);
}

extern "C" void kernel_launch(void* const* d_in, const int* in_sizes, int n_in, void* d_out, int out_size,
                              void* d_ws, size_t ws_size, hipStream_t stream) {
  const u16* x = (const u16*)d_in[0];
  const u16* wqkv = (const u16*)d_in[1];
  const u16* wo = (const u16*)d_in[2];
  const u16* wg = (const u16*)d_in[4];
  const u16* wu = (const u16*)d_in[5];
  const u16* wd = (const u16*)d_in[6];
  const u16* gw = (const u16*)d_in[7];
  char* ws = (char*)d_ws;
  u16* hT = (u16*)(ws + WS_HT);
  u16* Qb = (u16*)(ws + WS_QB);
  char* KV = ws + WS_KV;
  u16* oT = (u16*)(ws + WS_OT);
  u16* part = (u16*)(ws + WS_PART);
  float* gbuf = (float*)(ws + WS_G);
  u16* out = (u16*)d_out;

  gemm1_kernel<<<256, 512, 0, stream>>>(x, wg, wu, wqkv, hT, Qb, KV, gw, gbuf);
  attn_kernel<<<512, 512, 0, stream>>>(Qb, KV, oT);
  down_kernel<<<256, 256, 0, stream>>>(hT, wd, part);
  final_kernel<<<128, 256, 0, stream>>>(oT, wo, part, gbuf, out);
}
```

```cpp
#include <hip/hip_runtime.h>
#include <hip/hip_bf16.h>
#include <stdint.h>
#include <type_traits>

typedef unsigned short u16;
typedef __attribute__((ext_vector_type(8))) short bf16x8;
typedef __attribute__((ext_vector_type(4))) short s16x4;
typedef __attribute__((ext_vector_type(4))) float f32x4;
typedef __attribute__((ext_vector_type(16))) float f32x16;
typedef __attribute__((ext_vector_type(2))) float f32x2;
typedef __attribute__((ext_vector_type(2))) __bf16 bf16x2_t;
typedef __attribute__((ext_vector_type(2))) unsigned u32x2;
typedef __attribute__((ext_vector_type(4))) unsigned u32x4;

#define DI __device__ __forceinline__
#define LDS_AS __attribute__((address_space(3)))

constexpr int SEQ = 2048, DM = 576, FF = 4608, NH = 32, DH = 18, NQKV = 1728;

DI unsigned pack_bf16(float lo, float hi) {
  f32x2 f = {lo, hi};
  bf16x2_t b = __builtin_convertvector(f, bf16x2_t);
  return __builtin_bit_cast(unsigned, b);
}
DI float bf16r(float x) { return __uint_as_float(pack_bf16(x, x) << 16); }
DI float bf2f(u16 v) { return __uint_as_float(((unsigned)v) << 16); }

DI s16x4 tr_read(const char* lds_ptr) {
  return __builtin_amdgcn_ds_read_tr16_b64_v4i16((LDS_AS s16x4*)(lds_ptr));
}
DI void glds16(const void* gsrc, char* lds_dst) {
  __builtin_amdgcn_global_load_lds((const __attribute__((address_space(1))) void*)gsrc,
                                   (LDS_AS void*)lds_dst, 16, 0, 0);
}
DI int lane_id_opaque() {
  int l;
  asm volatile("v_mbcnt_lo_u32_b32 %0, -1, 0\n\tv_mbcnt_hi_u32_b32 %0, -1, %0" : "=v"(l));
  return l;
}
DI void glds16_asm(unsigned voff, const void* sbase, unsigned lds_dst) {
  unsigned keep;
  asm volatile("s_mov_b32 %0, m0\n\ts_mov_b32 m0, %3\n\ts_nop 0\n\tglobal_load_lds_dwordx4 %1, %2\n\ts_mov_b32 m0, %0"
               : "=&s"(keep) : "v"(voff), "s"(sbase), "s"(lds_dst) : "memory");
}
DI void touch_line(const void* gptr, unsigned lds_scratch) {
  unsigned keep;
  asm volatile("s_mov_b32 %0, m0\n\ts_mov_b32 m0, %2\n\ts_nop 0\n\tglobal_load_lds_dword %1, off\n\ts_mov_b32 m0, %0"
               : "=&s"(keep) : "v"(gptr), "s"(lds_scratch) : "memory");
}
DI void touch_line_s(unsigned voff, const void* sbase, unsigned lds_scratch) {
  unsigned keep;
  asm volatile("s_mov_b32 %0, m0\n\ts_mov_b32 m0, %3\n\ts_nop 0\n\tglobal_load_lds_dword %1, %2\n\ts_mov_b32 m0, %0"
               : "=&s"(keep) : "v"(voff), "s"(sbase), "s"(lds_scratch) : "memory");
}
#define STAMP(kid, i) do {} while (0)
#define STAMPR(kid, i) do {} while (0)
#define MFMA16(a, b, c) __builtin_amdgcn_mfma_f32_16x16x32_bf16((a), (b), (c), 0, 0, 0)
#define MFMA32(a, b, c) __builtin_amdgcn_mfma_f32_32x32x16_bf16((a), (b), (c), 0, 0, 0)

constexpr size_t WS_HT = 0;
constexpr size_t WS_QB = WS_HT + (size_t)FF * SEQ * 2;
constexpr int KVREC = 2432;
constexpr size_t WS_KV = WS_QB + (size_t)NH * SEQ * 32 * 2;
constexpr size_t WS_VF = WS_KV;
constexpr size_t WS_OT = WS_KV + (size_t)NH * 64 * KVREC;
constexpr size_t WS_PART = WS_OT + (size_t)DM * SEQ * 2;
constexpr size_t WS_G = WS_PART + (size_t)4 * SEQ * DM * 4;
constexpr size_t WS_END = WS_G + SEQ * 4;

constexpr int G1_XB = 256 * 64;
constexpr int G1_WB = 9216 * 2 + 5120;
constexpr int G1_STAGE = G1_XB + G1_WB;

template <bool ISV, int WN>
DI void gemm1_body(const u16* __restrict__ x, const u16* __restrict__ wg, const u16* __restrict__ wu,
                   const u16* __restrict__ wqkv, u16* __restrict__ hT, u16* __restrict__ Qb,
                   char* __restrict__ KV, const u16* __restrict__ gw, float* __restrict__ gout, char* lds,
                   int bm, int j, int w) {
  const int l = lane_id_opaque();
  const int wm = w >> 1;
  const int g = l >> 4;
  const int jq = j < 27 ? j : 26;

  unsigned xvo, guvo, evo;
  const char* ebase;
  int estep, edst;
  {
    int row = 32 * w + (l >> 2);
    int chunk = (l & 3) ^ ((-(l >> 4)) & 3);
    xvo = (unsigned)((bm * 256 + row) * DM + chunk * 8) * 2u;
    {
      int c = w * 64 + l;
      int lr = c / 18, ch = c - lr * 18;
      int krow = lr ^ ((lr & 8) >> 1);
      guvo = (unsigned)(krow * FF + j * 144 + ch * 8) * 2u;
    }
    if (w < 2) {
      int c = 8 * 64 + l;
      int lr = c / 18, ch = c - lr * 18;
      int krow = lr ^ ((lr & 8) >> 1);
      evo = (unsigned)(krow * FF + j * 144 + ch * 8) * 2u;
      ebase = (const char*)(w ? wu : wg);
      estep = 32 * FF * 2;
      edst = G1_XB + w * 9216 + 8 * 1024;
    } else {
      int n = w - 2;
      if (n > 4) n = 4;
      int c = n * 64 + l;
      int lr = c / 10, ch = c - lr * 10;
      if (ch > 7) ch = 7;
      int krow = lr ^ ((lr & 8) >> 1);
      evo = (unsigned)(krow * NQKV + jq * 64 + ch * 8) * 2u;
      ebase = (const char*)wqkv;
      estep = 32 * NQKV * 2;
      edst = G1_XB + 18432 + n * 1024;
    }
  }

  const unsigned lds_base = (unsigned)(size_t)(LDS_AS char*)lds;
  auto stage_piece = [&](int t, int p) {
    const unsigned sb = lds_base + (t & 3) * G1_STAGE;
    if (p < 2) {
      const char* xb = (const char*)x + t * 64 + p * 16 * DM * 2;
      glds16_asm(xvo, xb, sb + (32 * w + 16 * p) * 64);
    } else if (p < 4) {
      const char* gb = (const char*)((p & 1) ? wu : wg) + (size_t)t * (32 * FF * 2);
      glds16_asm(guvo, gb, sb + G1_XB + (p & 1) * 9216 + w * 1024);
    } else {
      glds16_asm(evo, ebase + (size_t)t * estep, sb + edst);
    }
  };

  f32x4 acc[11][4];
#pragma unroll
  for (int i = 0; i < 11; ++i)
#pragma unroll
    for (int r = 0; r < 4; ++r) acc[i][r] = (f32x4){0.f, 0.f, 0.f, 0.f};

  const int xoff = wm * 4096 + (l & 15) * 64 + ((g ^ ((-((l & 15) >> 2)) & 3)) << 4);
  const int wq = (l & 15) >> 2, wp = l & 3;
  const int lr0 = g * 8 + 4 * (0 ^ (g & 1)) + wq, lr1 = g * 8 + 4 * (1 ^ (g & 1)) + wq;
  const int woff0 = G1_XB + lr0 * 288 + wp * 8, woff1 = G1_XB + lr1 * 288 + wp * 8;
  const int qoff0 = G1_XB + 18432 + lr0 * 160 + wp * 8, qoff1 = G1_XB + 18432 + lr1 * 160 + wp * 8;

  auto load_w = [&](const char* sb, int i) -> bf16x8 {
    const bool isq = (i == 10) || (WN == 1 && (i == 4 || i == 9));
    s16x4 lo, hi;
    if (isq) {
      const int qct = WN == 0 ? 0 : (i == 4 ? 1 : (i == 9 ? 2 : 3));
      lo = tr_read(sb + qoff0 + qct * 32);
      hi = tr_read(sb + qoff1 + qct * 32);
    } else {
      const int off = (i < 5 ? (WN * 5 + i) * 32 : 9216 + (WN * 5 + i - 5) * 32);
      lo = tr_read(sb + woff0 + off);
      hi = tr_read(sb + woff1 + off);
    }
    return __builtin_shufflevector(lo, hi, 0, 1, 2, 3, 4, 5, 6, 7);
  };

  const char* tbase;
  unsigned tvo;
  int tstep;
  {
    const int xcd = j >> 2, idx = (bm << 2) | (j & 3);
    if (w < 2) {
      tbase = (const char*)(w ? wu : wg); tstep = 32 * FF * 2;
      tvo = (unsigned)(idx * (FF * 2) + xcd * 1152 + (l < 9 ? l : 8) * 128);
    } else if (w == 2) {
      int cb = xcd * 512 + (l & 3) * 128;
      if (cb > NQKV * 2 - 128) cb = NQKV * 2 - 128;
      tbase = (const char*)wqkv; tstep = 32 * NQKV * 2;
      tvo = (unsigned)(idx * (NQKV * 2) + cb);
    } else {
      tbase = (const char*)x; tstep = 64;
      tvo = (unsigned)((idx * 64 + l) * (DM * 2));
    }
  }
  auto touch_step = [&](int st) {
    if (st > 17) st = 17;
    touch_line_s(tvo, tbase + (size_t)st * tstep, lds_base + 4 * G1_STAGE);
  };

  bf16x8 xf[4], wfa, wfb;
  STAMPR(0, 14);
  STAMP(0, 0);
#pragma unroll
  for (int t = 0; t < 3; ++t)
#pragma unroll
    for (int p = 0; p < 5; ++p) stage_piece(t, p);
  STAMP(0, 12);
  if (w < 4) {
    touch_step(3); touch_step(4); touch_step(5);
    asm volatile("s_waitcnt vmcnt(13)" ::: "memory");
  } else {
    asm volatile("s_waitcnt vmcnt(10)" ::: "memory");
  }
  STAMP(0, 13);
  __builtin_amdgcn_s_barrier();
  asm volatile("" ::: "memory");
#pragma unroll
  for (int rt = 0; rt < 4; ++rt) xf[rt] = *(const bf16x8*)(lds + xoff + rt * 1024);
  wfa = load_w(lds, 0);
  wfb = load_w(lds, 1);
  int tl = 18;
  asm volatile("" : "+s"(tl));
#pragma clang loop unroll(disable)
  for (int t = 0; t < tl; ++t) {
    if (t >= 16) asm volatile("s_waitcnt vmcnt(0)" ::: "memory");
    else if (w < 4) asm volatile("s_waitcnt vmcnt(7)" ::: "memory");
    else asm volatile("s_waitcnt vmcnt(5)" ::: "memory");
    __builtin_amdgcn_s_barrier();
    asm volatile("" ::: "memory");
    if ((t & 1) == 0) STAMP(0, 1 + (t >> 1));
    const bool issue = t < 15;
    const char* sb = lds + (t & 3) * G1_STAGE;
    const char* sn = lds + ((t + 1) & 3) * G1_STAGE;
    bf16x8 xn[4];
#pragma unroll
    for (int i = 0; i < 11; ++i) {
      bf16x8 wf = wfa;
      wfa = wfb;
      if (i + 2 < 11) wfb = load_w(sb, i + 2);
      else wfb = load_w(sn, i + 2 - 11);
      if (i >= 5 && i < 9) xn[i - 5] = *(const bf16x8*)(sn + xoff + (i - 5) * 1024);
      if ((i & 1) == 0 && i < 10 && issue) stage_piece(t + 3, i >> 1);
      if (i == 10 && w < 4 && issue) touch_step(t + 6);
      const bool isq = (i == 10) || (WN == 1 && (i == 4 || i == 9));
#pragma unroll
      for (int rt = 0; rt < 4; ++rt) {
        if (ISV && isq) acc[i][rt] = MFMA16(xf[rt], wf, acc[i][rt]);
        else acc[i][rt] = MFMA16(wf, xf[rt], acc[i][rt]);
      }
      __builtin_amdgcn_sched_barrier(0);
    }
#pragma unroll
    for (int rt = 0; rt < 4; ++rt) xf[rt] = xn[rt];
  }

  STAMP(0, 10);
  __builtin_amdgcn_sched_barrier(0);
  {
  const int l = lane_id_opaque();
  const int g = l >> 4;
  const int tok0 = bm * 256 + wm * 64;
  constexpr int NP = WN == 0 ? 5 : 4;
#pragma unroll
  for (int i = 0; i < NP; ++i) {
    const int ct = WN * 5 + i;
    const int chunk = j * 18 + ct * 2 + (g >> 1);
#pragma unroll
    for (int rt = 0; rt < 4; ++rt) {
      float hv[4];
#pragma unroll
      for (int r = 0; r < 4; ++r) hv[r] = bf16r(acc[i][rt][r]) * bf16r(acc[i + 5][rt][r]);
      u32x2 o = {pack_bf16(hv[0], hv[1]), pack_bf16(hv[2], hv[3])};
      int token = tok0 + rt * 16 + (l & 15);
      *(u32x2*)(hT + ((size_t)chunk * SEQ + token) * 8 + (g & 1) * 4) = o;
    }
  }
  if (j >= 28) {
    const int tid = w * 64 + l;
    const int token = bm * 256 + (j - 28) * 64 + (tid >> 3), prt = tid & 7;
    const u16* xr = x + (size_t)token * DM + prt * 72;
    const u16* gr = gw + prt * 72;
    bf16x8 xv[9], gv[9];
#pragma unroll
    for (int i = 0; i < 9; ++i) { xv[i] = *(const bf16x8*)(xr + i * 8); gv[i] = *(const bf16x8*)(gr + i * 8); }
    float sum = 0.f;
#pragma unroll
    for (int i = 0; i < 9; ++i)
#pragma unroll
      for (int e = 0; e < 8; ++e) sum += bf16r(bf2f((u16)xv[i][e]) * bf2f((u16)gv[i][e]));
    sum += __shfl_xor(sum, 1);
    sum += __shfl_xor(sum, 2);
    sum += __shfl_xor(sum, 4);
    sum = bf16r(sum);
    if (prt == 0) gout[token] = bf16r(1.0f / (1.0f + __expf(-sum)));
  }
  if (j < 27) {
    constexpr int NQ = WN == 0 ? 1 : 3;
#pragma unroll
    for (int qi = 0; qi < NQ; ++qi) {
      const int ai = WN == 0 ? 10 : (qi == 0 ? 4 : (qi == 1 ? 9 : 10));
      const int qct = WN == 0 ? 0 : qi + 1;
      if (ISV) {
        int cv = j * 64 + qct * 16 + (l & 15) - 1152;
        int head = cv / 18, d = cv - head * 18;
#pragma unroll
        for (int rt = 0; rt < 4; ++rt) {
          int t16 = tok0 + rt * 16;
          int tile32 = t16 >> 5, s = rt & 1;
          char* dst = KV + (size_t)(head * 64 + tile32) * KVREC + 1024 + ((s * 2 + (g & 1)) * 19 + d) * 16 + (g >> 1) * 8;
          u32x2 o = {pack_bf16(acc[ai][rt][0], acc[ai][rt][1]), pack_bf16(acc[ai][rt][2], acc[ai][rt][3])};
          *(u32x2*)dst = o;
          if (d == 17) *(u32x2*)(dst + 16) = (u32x2){0x3F803F80u, 0x3F803F80u};
        }
      } else {
        const bool isQ = j < 9;
        const float sc = isQ ? 0.23570226039551584f * 1.4426950408889634f : 1.0f;
#pragma unroll
        for (int pr = 0; pr < 2; ++pr) {
          int c = j * 64 + qct * 16 + 4 * g + 2 * pr - (isQ ? 0 : 576);
          int head = c / 18, d = c - head * 18;
#pragma unroll
          for (int rt = 0; rt < 4; ++rt) {
            int token = tok0 + rt * 16 + (l & 15);
            unsigned v = pack_bf16(acc[ai][rt][2 * pr] * sc, acc[ai][rt][2 * pr + 1] * sc);
            char* dst;
            if (isQ) dst = (char*)(Qb + ((size_t)head * SEQ + token) * 32 + d);
            else {
              char* rec = KV + (size_t)(head * 64 + (token >> 5)) * KVREC;
              dst = d < 16 ? rec + (token & 31) * 32 + d * 2 : rec + 2240 + (token & 31) * 4;
            }
            *(unsigned*)dst = v;
          }
        }
      }
    }
  }
  }
  STAMP(0, 11);
  STAMPR(0, 15);
}

__global__ __launch_bounds__(512, 2) void gemm1_kernel(const u16* __restrict__ x, const u16* __restrict__ wg,
                                                       const u16* __restrict__ wu, const u16* __restrict__ wqkv,
                                                       u16* __restrict__ hT, u16* __restrict__ Qb,
                                                       char* __restrict__ KV, const u16* __restrict__ gw,
                                                       float* __restrict__ gout) {
  __shared__ __attribute__((aligned(16))) char lds[4 * G1_STAGE + 256];
  const int b = blockIdx.x;
  const int xcd = b & 7, idx = b >> 3;
  const int j = xcd * 4 + (idx & 3), bm = idx >> 2;
  const int w = __builtin_amdgcn_readfirstlane(threadIdx.x >> 6);
  const bool isv = (j >= 18);
  if (w & 1) {
    if (isv) gemm1_body<true, 1>(x, wg, wu, wqkv, hT, Qb, KV, gw, gout, lds, bm, j, w);
    else gemm1_body<false, 1>(x, wg, wu, wqkv, hT, Qb, KV, gw, gout, lds, bm, j, w);
  } else {
    if (isv) gemm1_body<true, 0>(x, wg, wu, wqkv, hT, Qb, KV, gw, gout, lds, bm, j, w);
    else gemm1_body<false, 0>(x, wg, wu, wqkv, hT, Qb, KV, gw, gout, lds, bm, j, w);
  }
}

DI float xhalf_max(float v) {
  auto rr = __builtin_amdgcn_permlane32_swap(__float_as_uint(v), __float_as_uint(v), false, false);
  return fmaxf(__uint_as_float(rr[0]), __uint_as_float(rr[1]));
}
DI float tile_max(const f32x16& S) {
  float t = fmaxf(fmaxf(S[0], S[1]), S[2]);
#pragma unroll
  for (int i = 3; i < 15; i += 2) t = fmaxf(fmaxf(t, S[i]), S[i + 1]);
  return xhalf_max(fmaxf(t, S[15]));
}
DI f32x16 qk_tile(bf16x8 kA, unsigned kH, bf16x8 qA, u32x4 qB) {
  f32x16 S;
#pragma unroll
  for (int i = 0; i < 16; ++i) S[i] = 0.f;
  u32x4 kBv = {kH, 0x00003F80u, 0u, 0u};
  S = MFMA32(kA, qA, S);
  S = MFMA32(__builtin_bit_cast(bf16x8, kBv), __builtin_bit_cast(bf16x8, qB), S);
  return S;
}
DI void pv_tile(f32x16& S, bf16x8 v0, bf16x8 v1, f32x16& O) {
#pragma unroll
  for (int i = 0; i < 16; ++i) S[i] = __builtin_amdgcn_exp2f(S[i]);
  u32x4 p0 = {pack_bf16(S[0], S[1]), pack_bf16(S[2], S[3]), pack_bf16(S[4], S[5]), pack_bf16(S[6], S[7])};
  u32x4 p1 = {pack_bf16(S[8], S[9]), pack_bf16(S[10], S[11]), pack_bf16(S[12], S[13]), pack_bf16(S[14], S[15])};
  O = MFMA32(v0, __builtin_bit_cast(bf16x8, p0), O);
  O = MFMA32(v1, __builtin_bit_cast(bf16x8, p1), O);
}

constexpr int AT_STAGE = 2 * 8 * KVREC;

DI void attn_body(const u16* __restrict__ Qb, const char* __restrict__ KV, u16* __restrict__ oT, char* lds,
                  const int b) {
  const int head = (b & 7) * 4 + ((b >> 3) & 3), qblk = b >> 5;
  const int w = __builtin_amdgcn_readfirstlane(threadIdx.x >> 6);
  const int l = threadIdx.x & 63, r = l & 31, h = l >> 5;
  const int qp = w & 3, kvh = w >> 2;
  const int token0 = qblk * 256 + qp * 64 + r;

  const unsigned lds_base = (unsigned)(size_t)(LDS_AS char*)lds;
  const char* kvsrc = KV + (size_t)(head * 64) * KVREC;
  auto stage = [&](int s) {
#pragma unroll
    for (int i = 0; i < 5; ++i) {
      int n = w * 5 + i;
      if (n > 37) n -= 2;
      const int hf = n >= 19 ? 1 : 0, pc = n - hf * 19;
      glds16_asm((unsigned)(l * 16), kvsrc + (hf * 32 + s * 8) * KVREC + pc * 1024,
                 lds_base + (s & 1) * AT_STAGE + hf * (8 * KVREC) + pc * 1024);
    }
  };
  STAMPR(1, 14);
  STAMP(1, 0);
  stage(0);

  bf16x8 qA[2];
  u32x4 qB[2];
  float m[2];
  f32x16 O[2];
#pragma unroll
  for (int q = 0; q < 2; ++q) {
    const u16* qrow = Qb + ((size_t)head * SEQ + token0 + q * 32) * 32;
    qA[q] = *(const bf16x8*)(qrow + 8 * h);
    unsigned q67 = *(const unsigned*)(qrow + 16);
    asm volatile("" : "+v"(qA[q]), "+v"(q67));
    qB[q] = (u32x4){h ? 0u : q67, 0u, 0u, 0u};
    m[q] = 0.f;
#pragma unroll
    for (int i = 0; i < 16; ++i) O[q][i] = 0.f;
  }
  bool failed = false;

  const int koff = kvh * (8 * KVREC) + r * 32 + h * 16;
  const int hoff = kvh * (8 * KVREC) + 2240 + r * 4;
  const int voff = kvh * (8 * KVREC) + 1024 + (h * 19 + (r < 18 ? r : 18)) * 16;

#pragma clang loop unroll(disable)
  for (int s = 0; s < 4; ++s) {
    asm volatile("s_waitcnt vmcnt(0)" ::: "memory");
    __builtin_amdgcn_s_barrier();
    asm volatile("" ::: "memory");
    STAMP(1, 1 + s);
    if (s < 3) stage(s + 1);
    const char* sb = lds + (s & 1) * AT_STAGE;
    bf16x8 kA = *(const bf16x8*)(sb + koff);
    unsigned kH = *(const unsigned*)(sb + hoff);
    if (s == 0) {
#pragma unroll
      for (int q = 0; q < 2; ++q) {
        f32x16 S0 = qk_tile(kA, kH, qA[q], qB[q]);
        m[q] = bf16r(tile_max(S0));
        qB[q][1] = h ? 0u : pack_bf16(-m[q], 0.f);
      }
    }
    f32x16 Sa = qk_tile(kA, kH, qA[0], qB[0]);
#pragma unroll
    for (int i = 0; i < 8; ++i) {
      bf16x8 v0 = *(const bf16x8*)(sb + voff + i * KVREC);
      bf16x8 v1 = *(const bf16x8*)(sb + voff + i * KVREC + 608);
      f32x16 Sb = qk_tile(kA, kH, qA[1], qB[1]);
      if (i < 7) {
        kA = *(const bf16x8*)(sb + koff + (i + 1) * KVREC);
        kH = *(const unsigned*)(sb + hoff + (i + 1) * KVREC);
      }
      pv_tile(Sa, v0, v1, O[0]);
      if (i < 7) Sa = qk_tile(kA, kH, qA[0], qB[0]);
      pv_tile(Sb, v0, v1, O[1]);
    }
    float lmax = h ? 0.f : fmaxf(O[0][10], O[1][10]);
    if (__any(!(lmax < 4294967296.f) || !(O[0][10] == O[0][10]) || !(O[1][10] == O[1][10]))) {
#pragma unroll
      for (int q = 0; q < 2; ++q) {
        float lv = O[q][10];
        auto rr = __builtin_amdgcn_permlane32_swap(__float_as_uint(lv), __float_as_uint(lv), false, false);
        lv = __uint_as_float(rr[0]);
        if (__any(!(lv < 3.0e38f))) failed = true;
        float sh = lv > 1.f && lv < 3.0e38f ? floorf(__builtin_amdgcn_logf(lv)) : 0.f;
        float mn = bf16r(m[q] + sh);
        float sc = __builtin_amdgcn_exp2f(m[q] - mn);
        m[q] = mn;
#pragma unroll
        for (int i = 0; i < 16; ++i) O[q][i] *= sc;
        qB[q][1] = h ? 0u : pack_bf16(-mn, 0.f);
      }
    }
  }

  STAMP(1, 5);
  if (failed) {
#pragma clang loop unroll(disable)
    for (int q = 0; q < 2; ++q) {
      const char* rec = KV + (size_t)(head * 64 + kvh * 32) * KVREC;
      const bf16x8 qa = q ? qA[1] : qA[0];
      u32x4 qb = q ? qB[1] : qB[0];
      qb[1] = 0u;
      float mm = -3.0e38f;
      f32x16 Oo;
#pragma unroll
      for (int i = 0; i < 16; ++i) Oo[i] = 0.f;
#pragma clang loop unroll(disable)
      for (int it = 0; it < 32; ++it, rec += KVREC) {
        bf16x8 kA = *(const bf16x8*)(rec + r * 32 + h * 16);
        unsigned kH = *(const unsigned*)(rec + 2240 + r * 4);
        bf16x8 v0 = *(const bf16x8*)(rec + 1024 + (h * 19 + (r < 18 ? r : 18)) * 16);
        bf16x8 v1 = *(const bf16x8*)(rec + 1024 + 608 + (h * 19 + (r < 18 ? r : 18)) * 16);
        f32x16 S = qk_tile(kA, kH, qa, qb);
        float mn = fmaxf(mm, tile_max(S));
        float sc = __builtin_amdgcn_exp2f(mm - mn);
        mm = mn;
#pragma unroll
        for (int i = 0; i < 16; ++i) { S[i] -= mn; Oo[i] *= sc; }
        pv_tile(S, v0, v1, Oo);
      }
      if (q) { O[1] = Oo; m[1] = mm; } else { O[0] = Oo; m[0] = mm; }
    }
  }

  __syncthreads();
  float* mrg = (float*)lds;
  if (kvh == 1) {
#pragma unroll
    for (int q = 0; q < 2; ++q) {
#pragma unroll
      for (int i = 0; i < 12; ++i) mrg[((qp * 2 + q) * 13 + i) * 64 + l] = O[q][i];
      mrg[((qp * 2 + q) * 13 + 12) * 64 + l] = m[q];
    }
  }
  __syncthreads();
  if (kvh == 0) {
#pragma unroll
    for (int q = 0; q < 2; ++q) {
      float m1 = mrg[((qp * 2 + q) * 13 + 12) * 64 + l];
      float mm = fmaxf(m[q], m1);
      float a0 = __builtin_amdgcn_exp2f(m[q] - mm), a1 = __builtin_amdgcn_exp2f(m1 - mm);
      float o[12];
#pragma unroll
      for (int i = 0; i < 12; ++i) o[i] = O[q][i] * a0 + mrg[((qp * 2 + q) * 13 + i) * 64 + l] * a1;
      float lsum = __shfl(o[10], r);
      float inv = 1.0f / lsum;
      const int token = token0 + q * 32;
#pragma unroll
      for (int pr = 0; pr < 5; ++pr) {
        if (pr == 4 && h) continue;
        int d = (pr >> 1) * 8 + (pr & 1) * 2 + 4 * h;
        int c = head * 18 + d;
        unsigned v = pack_bf16(o[2 * pr] * inv, o[2 * pr + 1] * inv);
        *(unsigned*)(oT + ((size_t)(c >> 3) * SEQ + token) * 8 + (c & 7)) = v;
      }
    }
  }
  STAMP(1, 6);
  STAMPR(1, 15);
}

template <int N> DI void wait_vm() { asm volatile("s_waitcnt vmcnt(%0)" ::"i"(N) : "memory"); }
DI void raw_barrier() {
  __builtin_amdgcn_s_barrier();
  asm volatile("" ::: "memory");
}

template <int NW, int RT, int NS = 4>
struct Gemm2 {
  static constexpr int BM = NW * RT * 16;
  static constexpr int AB = 8 * BM * 16;
  static constexpr int STG = AB + 18432;
  static constexpr int NAW = AB / 1024 / NW;
  static constexpr int NWW = (18 + NW - 1) / NW;
  static constexpr int NV = NAW + NWW;
  static constexpr int LA = 4;

  const char* abase;
  const char* wbase;
  unsigned avo[NAW], wvo[NWW];
  unsigned lds_base;
  char* lds;
  int w, aoff, woff0, woff1;
  bf16x8 af[2][RT], wq_[LA];

  DI void init(const u16* AT, const u16* W, int tok0, int col0, int kbeg, char* lds_, int w_, int l) {
    lds = lds_; w = w_;
    lds_base = (unsigned)(size_t)(LDS_AS char*)lds_;
    abase = (const char*)(AT + (size_t)(kbeg / 8) * SEQ * 8);
    wbase = (const char*)(W + (size_t)kbeg * DM);
    const int g = l >> 4;
#pragma unroll
    for (int i = 0; i < NAW; ++i) {
      int off = (w * NAW + i) * 64 + l;
      int chunk = off / BM, token = off - chunk * BM;
      avo[i] = (unsigned)((chunk * SEQ + tok0 + token) * 16);
    }
#pragma unroll
    for (int i = 0; i < NWW; ++i) {
      int n = w + NW * i;
      if (n >= 18) n = 16 + (n & 1);
      int c = n * 64 + l;
      int lr = c / 18, ch = c - lr * 18;
      int krow = lr ^ ((lr & 8) >> 1);
      wvo[i] = (unsigned)((krow * DM + col0 + ch * 8) * 2);
    }
    aoff = g * (BM * 16) + (w * RT * 16 + (l & 15)) * 16;
    const int wq = (l & 15) >> 2, wp = l & 3;
    woff0 = AB + (g * 8 + 4 * (0 ^ (g & 1)) + wq) * 288 + wp * 8;
    woff1 = AB + (g * 8 + 4 * (1 ^ (g & 1)) + wq) * 288 + wp * 8;
  }
  DI void piece(int t, int p) {
    const unsigned sb = lds_base + (t & (NS - 1)) * STG;
    if (p < NAW) {
      glds16_asm(avo[p], abase + (size_t)t * (8 * SEQ * 16), sb + (w * NAW + p) * 1024);
    } else {
      const int i = p - NAW;
      int n = w + NW * i;
      if (n >= 18) n = 16 + (n & 1);
      glds16_asm(wvo[i], wbase + (size_t)t * (64 * DM * 2), sb + AB + n * 1024);
    }
  }
  DI bf16x8 load_w(const char* sb, int grp) {
    const int kk = grp / 9, ct = grp - kk * 9;
    s16x4 lo = tr_read(sb + woff0 + kk * 9216 + ct * 32);
    s16x4 hi = tr_read(sb + woff1 + kk * 9216 + ct * 32);
    return __builtin_shufflevector(lo, hi, 0, 1, 2, 3, 4, 5, 6, 7);
  }
  DI bf16x8 load_a(const char* sb, int kk, int rt) {
    return *(const bf16x8*)(sb + aoff + kk * 4 * BM * 16 + rt * 256);
  }
  DI void prologue_issue() {
#pragma unroll
    for (int t = 0; t < 3; ++t)
#pragma unroll
      for (int p = 0; p < NV; ++p) piece(t, p);
  }
  DI void prologue_read() {
    wait_vm<2 * NV>();
    raw_barrier();
#pragma unroll
    for (int kk = 0; kk < 2; ++kk)
#pragma unroll
      for (int rt = 0; rt < RT; ++rt) af[kk][rt] = load_a(lds, kk, rt);
#pragma unroll
    for (int i = 0; i < LA; ++i) wq_[i] = load_w(lds, i);
  }
  template <bool ISSUE, bool PREF>
  DI void body(int t, f32x4 (&acc)[9][RT]) {
    const char* sb = lds + (t & 3) * STG;
    const char* sn = lds + ((t + 1) & 3) * STG;
    bf16x8 an[2][RT];
#pragma unroll
    for (int grp = 0; grp < 18; ++grp) {
      bf16x8 wf = wq_[0];
#pragma unroll
      for (int i = 0; i + 1 < LA; ++i) wq_[i] = wq_[i + 1];
      if (grp + LA < 18) wq_[LA - 1] = load_w(sb, grp + LA);
      else if (PREF) wq_[LA - 1] = load_w(sn, grp + LA - 18);
      if (PREF && grp >= 8 && grp < 8 + 2 * RT) an[(grp - 8) / RT][(grp - 8) % RT] = load_a(sn, (grp - 8) / RT, (grp - 8) % RT);
      if (ISSUE && grp < NV) piece(t + 3, grp);
      const int kk = grp / 9, ct = grp - kk * 9;
#pragma unroll
      for (int rt = 0; rt < RT; ++rt) acc[ct][rt] = MFMA16(wf, af[kk][rt], acc[ct][rt]);
      __builtin_amdgcn_sched_barrier(0);
    }
    if (PREF) {
#pragma unroll
      for (int kk = 0; kk < 2; ++kk)
#pragma unroll
        for (int rt = 0; rt < RT; ++rt) af[kk][rt] = an[kk][rt];
    }
  }
  template <bool ISSUE>
  DI void body2(int t, f32x4 (&acc)[9][RT]) {
    const char* sb = lds + (t & 1) * STG;
#pragma unroll
    for (int kk = 0; kk < 2; ++kk)
#pragma unroll
      for (int rt = 0; rt < RT; ++rt) af[kk][rt] = load_a(sb, kk, rt);
#pragma unroll
    for (int i = 0; i < LA; ++i) wq_[i] = load_w(sb, i);
#pragma unroll
    for (int grp = 0; grp < 18; ++grp) {
      bf16x8 wf = wq_[0];
#pragma unroll
      for (int i = 0; i + 1 < LA; ++i) wq_[i] = wq_[i + 1];
      if (grp + LA < 18) wq_[LA - 1] = load_w(sb, grp + LA);
      if (ISSUE && grp < NV) piece(t + 1, grp);
      const int kk = grp / 9, ct = grp - kk * 9;
#pragma unroll
      for (int rt = 0; rt < RT; ++rt) acc[ct][rt] = MFMA16(wf, af[kk][rt], acc[ct][rt]);
      __builtin_amdgcn_sched_barrier(0);
    }
  }
  DI void run2(int nsteps, f32x4 (&acc)[9][RT]) {
    int tl = nsteps - 1;
    asm volatile("" : "+s"(tl));
#pragma clang loop unroll(disable)
    for (int t = 0; t < tl; ++t) {
      wait_vm<0>();
      raw_barrier();
      body2<true>(t, acc);
    }
    wait_vm<0>();
    raw_barrier();
    body2<false>(tl, acc);
  }
  DI void run(int nsteps, f32x4 (&acc)[9][RT]) {
    prologue_read();
    int tl = nsteps - 3;
    asm volatile("" : "+s"(tl));
#pragma clang loop unroll(disable)
    for (int t = 0; t < tl; ++t) {
      wait_vm<NV>();
      raw_barrier();
      body<true, true>(t, acc);
    }
    wait_vm<NV>();
    raw_barrier();
    body<false, true>(tl, acc);
    wait_vm<0>();
    raw_barrier();
    body<false, true>(tl + 1, acc);
    body<false, false>(tl + 2, acc);
  }
};

DI void down_body(const u16* __restrict__ hT, const u16* __restrict__ wd, u16* __restrict__ part, char* lds,
                  const int b) {
  using G = Gemm2<8, 1, 2>;
  const int xcd = b & 7, idx = b >> 3;
  const int ks = xcd >> 1;
  const int bm = (xcd & 1) * 8 + (idx >> 2);
  const int bn = idx & 3;
  const int w = __builtin_amdgcn_readfirstlane(threadIdx.x >> 6);
  const int l = threadIdx.x & 63;
  STAMPR(2, 14);
  STAMP(2, 0);
  G gm;
  gm.init(hT, wd, bm * 128, bn * 144, ks * 1152, lds, w, l);
#pragma unroll
  for (int p = 0; p < G::NV; ++p) gm.piece(0, p);
  {
    const int Gi = idx * 512 + threadIdx.x;
    const unsigned scr = gm.lds_base + 2 * G::STG;
    const char* hb = (const char*)hT + ((size_t)(ks * 144) * SEQ + (xcd & 1) * 1024) * 16;
    touch_line(hb + (size_t)(Gi >> 7) * (SEQ * 16) + (Gi & 127) * 128, scr);
    const int G2 = 16384 + (Gi & 2047);
    touch_line(hb + (size_t)(G2 >> 7) * (SEQ * 16) + (G2 & 127) * 128, scr);
    const int G3 = Gi < 10368 ? Gi : 10367;
    touch_line((const char*)wd + (size_t)ks * (1152 * DM * 2) + (size_t)G3 * 128, scr);
  }
  STAMP(2, 1);
  f32x4 acc[9][1];
#pragma unroll
  for (int i = 0; i < 9; ++i) acc[i][0] = (f32x4){0.f, 0.f, 0.f, 0.f};
  gm.run2(18, acc);
  STAMP(2, 2);
#pragma unroll
  for (int ct = 0; ct < 9; ++ct) {
    int tt = bm * 8 + w, ctg = bn * 9 + ct;
    u32x2 pv = {pack_bf16(acc[ct][0][0], acc[ct][0][1]), pack_bf16(acc[ct][0][2], acc[ct][0][3])};
    *(u32x2*)(part + ((size_t)(ks * 128 + tt) * 36 + ctg) * 256 + l * 4) = pv;
  }
  STAMP(2, 3);
  STAMPR(2, 15);
}

__global__ __launch_bounds__(512, 4) void mid_kernel(const u16* __restrict__ Qb, const char* __restrict__ KV,
                                                     u16* __restrict__ oT, const u16* __restrict__ hT,
                                                     const u16* __restrict__ wd, u16* __restrict__ part) {
  __shared__ __attribute__((aligned(16))) char lds[2 * AT_STAGE];
  const int b = blockIdx.x;
  if (b >= 256) down_body(hT, wd, part, lds, b - 256);
  else attn_body(Qb, KV, oT, lds, b);
}

__global__ __launch_bounds__(256, 1) void final_kernel(const u16* __restrict__ oT, const u16* __restrict__ wo,
                                                       const u16* __restrict__ part, const float* __restrict__ gin,
                                                       u16* __restrict__ out) {
  using G = Gemm2<4, 1>;
  __shared__ __attribute__((aligned(16))) char lds[4 * G::STG];
  const int b = blockIdx.x;
  const int bn = b & 3, bm = b >> 2;
  const int w = __builtin_amdgcn_readfirstlane(threadIdx.x >> 6);
  const int l = threadIdx.x & 63;
  STAMPR(3, 14);
  STAMP(3, 0);
  G gm;
  gm.init(oT, wo, bm * 64, bn * 144, 0, lds, w, l);
  gm.prologue_issue();
  STAMP(3, 1);

  const int tt = bm * 4 + w;
  const int token = bm * 64 + w * 16 + (l & 15);
  float gg = gin[token];
  f32x4 bsum[9];
#pragma unroll
  for (int ct = 0; ct < 9; ++ct) {
    int ctg = bn * 9 + ct;
    f32x4 pp[4];
#pragma unroll
    for (int ks = 0; ks < 4; ++ks) {
      u32x2 v = *(const u32x2*)(part + ((size_t)(ks * 128 + tt) * 36 + ctg) * 256 + l * 4);
      pp[ks] = (f32x4){__uint_as_float(v[0] << 16), __uint_as_float(v[0] & 0xffff0000u),
                       __uint_as_float(v[1] << 16), __uint_as_float(v[1] & 0xffff0000u)};
    }
    bsum[ct] = (pp[0] + pp[1]) + (pp[2] + pp[3]);
  }
#pragma unroll
  for (int ct = 0; ct < 9; ++ct) asm volatile("" : "+v"(bsum[ct]));
  asm volatile("" : "+v"(gg));
  STAMP(3, 2);
  f32x4 acc[9][1];
#pragma unroll
  for (int i = 0; i < 9; ++i) acc[i][0] = (f32x4){0.f, 0.f, 0.f, 0.f};
  gm.run(9, acc);
  STAMP(3, 3);
  const float og = bf16r(1.0f - gg);
#pragma unroll
  for (int ct = 0; ct < 9; ++ct) {
    int ctg = bn * 9 + ct;
    float o[4];
#pragma unroll
    for (int e = 0; e < 4; ++e) o[e] = bf16r(gg * bf16r(acc[ct][0][e])) + bf16r(og * bf16r(bsum[ct][e]));
    u32x2 ov = {pack_bf16(o[0], o[1]), pack_bf16(o[2], o[3])};
    *(u32x2*)(out + (size_t)token * DM + ctg * 16 + (l >> 4) * 4) = ov;
  }
  STAMP(3, 4);
  STAMPR(3, 15);
}

extern "C" void kernel_launch(void* const* d_in, const int* in_sizes, int n_in, void* d_out, int out_size,
                              void* d_ws, size_t ws_size, hipStream_t stream) {
  const u16* x = (const u16*)d_in[0];
  const u16* wqkv = (const u16*)d_in[1];
  const u16* wo = (const u16*)d_in[2];
  const u16* wg = (const u16*)d_in[4];
  const u16* wu = (const u16*)d_in[5];
  const u16* wd = (const u16*)d_in[6];
  const u16* gw = (const u16*)d_in[7];
  char* ws = (char*)d_ws;
  u16* hT = (u16*)(ws + WS_HT);
  u16* Qb = (u16*)(ws + WS_QB);
  char* KV = ws + WS_KV;
  u16* oT = (u16*)(ws + WS_OT);
  u16* part = (u16*)(ws + WS_PART);
  float* gbuf = (float*)(ws + WS_G);
  u16* out = (u16*)d_out;

  gemm1_kernel<<<256, 512, 0, stream>>>(x, wg, wu, wqkv, hT, Qb, KV, gw, gbuf);
  mid_kernel<<<512, 512, 0, stream>>>(Qb, KV, oT, hT, wd, part);
  final_kernel<<<128, 256, 0, stream>>>(oT, wo, part, gbuf, out);
}
```

```cpp
#include <hip/hip_runtime.h>
#include <hip/hip_bf16.h>
#include <stdint.h>
#include <type_traits>

typedef unsigned short u16;
typedef __attribute__((ext_vector_type(8))) short bf16x8;
typedef __attribute__((ext_vector_type(4))) short s16x4;
typedef __attribute__((ext_vector_type(4))) float f32x4;
typedef __attribute__((ext_vector_type(16))) float f32x16;
typedef __attribute__((ext_vector_type(2))) float f32x2;
typedef __attribute__((ext_vector_type(2))) __bf16 bf16x2_t;
typedef __attribute__((ext_vector_type(2))) unsigned u32x2;
typedef __attribute__((ext_vector_type(4))) unsigned u32x4;

#define DI __device__ __forceinline__
#define LDS_AS __attribute__((address_space(3)))

constexpr int SEQ = 2048, DM = 576, FF = 4608, NH = 32, DH = 18, NQKV = 1728;

DI unsigned pack_bf16(float lo, float hi) {
  f32x2 f = {lo, hi};
  bf16x2_t b = __builtin_convertvector(f, bf16x2_t);
  return __builtin_bit_cast(unsigned, b);
}
DI float bf16r(float x) { return __uint_as_float(pack_bf16(x, x) << 16); }
DI float bf2f(u16 v) { return __uint_as_float(((unsigned)v) << 16); }

DI s16x4 tr_read(const char* lds_ptr) {
  return __builtin_amdgcn_ds_read_tr16_b64_v4i16((LDS_AS s16x4*)(lds_ptr));
}
DI void glds16(const void* gsrc, char* lds_dst) {
  __builtin_amdgcn_global_load_lds((const __attribute__((address_space(1))) void*)gsrc,
                                   (LDS_AS void*)lds_dst, 16, 0, 0);
}
DI int lane_id_opaque() {
  int l;
  asm volatile("v_mbcnt_lo_u32_b32 %0, -1, 0\n\tv_mbcnt_hi_u32_b32 %0, -1, %0" : "=v"(l));
  return l;
}
DI void glds16_asm(unsigned voff, const void* sbase, unsigned lds_dst) {
  unsigned keep;
  asm volatile("s_mov_b32 %0, m0\n\ts_mov_b32 m0, %3\n\ts_nop 0\n\tglobal_load_lds_dwordx4 %1, %2\n\ts_mov_b32 m0, %0"
               : "=&s"(keep) : "v"(voff), "s"(sbase), "s"(lds_dst) : "memory");
}
DI void touch_line(const void* gptr, unsigned lds_scratch) {
  unsigned keep;
  asm volatile("s_mov_b32 %0, m0\n\ts_mov_b32 m0, %2\n\ts_nop 0\n\tglobal_load_lds_dword %1, off\n\ts_mov_b32 m0, %0"
               : "=&s"(keep) : "v"(gptr), "s"(lds_scratch) : "memory");
}
DI void touch_line_s(unsigned voff, const void* sbase, unsigned lds_scratch) {
  unsigned keep;
  asm volatile("s_mov_b32 %0, m0\n\ts_mov_b32 m0, %3\n\ts_nop 0\n\tglobal_load_lds_dword %1, %2\n\ts_mov_b32 m0, %0"
               : "=&s"(keep) : "v"(voff), "s"(sbase), "s"(lds_scratch) : "memory");
}
#define STAMP(kid, i) do {} while (0)
#define STAMPR(kid, i) do {} while (0)
#define MFMA16(a, b, c) __builtin_amdgcn_mfma_f32_16x16x32_bf16((a), (b), (c), 0, 0, 0)
#define MFMA32(a, b, c) __builtin_amdgcn_mfma_f32_32x32x16_bf16((a), (b), (c), 0, 0, 0)

constexpr size_t WS_HT = 0;
constexpr size_t WS_QB = WS_HT + (size_t)FF * SEQ * 2;
constexpr int KVREC = 2432;
constexpr size_t WS_KV = WS_QB + (size_t)NH * SEQ * 32 * 2;
constexpr size_t WS_VF = WS_KV;
constexpr size_t WS_OT = WS_KV + (size_t)NH * 64 * KVREC;
constexpr size_t WS_PART = WS_OT + (size_t)DM * SEQ * 2;
constexpr size_t WS_G = WS_PART + (size_t)4 * SEQ * DM * 4;
constexpr size_t WS_END = WS_G + SEQ * 4;

constexpr int G1_XB = 256 * 128;
constexpr int G1_WB = 18432 * 2 + 10240;
constexpr int G1_STAGE = G1_XB + G1_WB;

template <bool ISV, int WN>
DI void gemm1_body(const u16* __restrict__ x, const u16* __restrict__ wg, const u16* __restrict__ wu,
                   const u16* __restrict__ wqkv, u16* __restrict__ hT, u16* __restrict__ Qb,
                   char* __restrict__ KV, const u16* __restrict__ gw, float* __restrict__ gout, char* lds,
                   int bm, int j, int w) {
  const int l = lane_id_opaque();
  const int wm = w >> 1;
  const int g = l >> 4;
  const int jq = j < 27 ? j : 26;

  unsigned xvo, guvo[2], evo[2];
  const char* ebase[2];
  int estep, edst[2];
  {
    int row = 32 * w + (l >> 3);
    int chunk = (l & 7) ^ (l >> 3);
    xvo = (unsigned)((bm * 256 + row) * DM + chunk * 8) * 2u;
#pragma unroll
    for (int i = 0; i < 2; ++i) {
      int c = (w + 8 * i) * 64 + l;
      int lr = c / 18, ch = c - lr * 18;
      int krow = lr ^ ((lr & 8) >> 1);
      guvo[i] = (unsigned)(krow * FF + j * 144 + ch * 8) * 2u;
    }
    if (w < 2) {
      int c = (16 + w) * 64 + l;
      int lr = c / 18, ch = c - lr * 18;
      int krow = lr ^ ((lr & 8) >> 1);
      evo[0] = evo[1] = (unsigned)(krow * FF + j * 144 + ch * 8) * 2u;
      ebase[0] = (const char*)wg; ebase[1] = (const char*)wu;
      estep = 64 * FF * 2;
      edst[0] = G1_XB + (16 + w) * 1024; edst[1] = G1_XB + 18432 + (16 + w) * 1024;
    } else {
#pragma unroll
      for (int i = 0; i < 2; ++i) {
        int n = (w - 2) + 6 * i;
        if (n > 9) n = 9;
        int c = n * 64 + l;
        int lr = c / 10, ch = c - lr * 10;
        if (ch > 7) ch = 7;
        int krow = lr ^ ((lr & 8) >> 1);
        evo[i] = (unsigned)(krow * NQKV + jq * 64 + ch * 8) * 2u;
        ebase[i] = (const char*)wqkv;
        edst[i] = G1_XB + 36864 + n * 1024;
      }
      estep = 64 * NQKV * 2;
    }
    edst[0] = __builtin_amdgcn_readfirstlane(edst[0]);
    edst[1] = __builtin_amdgcn_readfirstlane(edst[1]);
  }

  const unsigned lds_base = (unsigned)(size_t)(LDS_AS char*)lds;
  auto stage_piece = [&](int t, int p) {
    const unsigned sb = lds_base + (t & 1) * G1_STAGE;
    if (p < 4) {
      const char* xb = (const char*)x + t * 128 + p * 8 * DM * 2;
      glds16_asm(xvo, xb, sb + (32 * w + 8 * p) * 128);
    } else if (p < 8) {
      const int i = (p - 4) >> 1;
      const char* gb = (const char*)((p & 1) ? wu : wg) + (size_t)t * (64 * FF * 2);
      glds16_asm(guvo[i], gb, sb + G1_XB + (p & 1) * 18432 + (w + 8 * i) * 1024);
    } else {
      glds16_asm(evo[p & 1], ebase[p & 1] + (size_t)t * estep, sb + edst[p & 1]);
    }
  };

  f32x4 acc[11][4];
#pragma unroll
  for (int i = 0; i < 11; ++i)
#pragma unroll
    for (int r = 0; r < 4; ++r) acc[i][r] = (f32x4){0.f, 0.f, 0.f, 0.f};

  const int xoff0 = wm * 8192 + (l & 15) * 128 + (((0 + g) ^ (l & 7)) << 4);
  const int xoff1 = wm * 8192 + (l & 15) * 128 + (((4 + g) ^ (l & 7)) << 4);
  const int wq = (l & 15) >> 2, wp = l & 3;
  const int lr0 = g * 8 + 4 * (0 ^ (g & 1)) + wq, lr1 = g * 8 + 4 * (1 ^ (g & 1)) + wq;
  const int woff0 = G1_XB + lr0 * 288 + wp * 8, woff1 = G1_XB + lr1 * 288 + wp * 8;
  const int qoff0 = G1_XB + 36864 + lr0 * 160 + wp * 8, qoff1 = G1_XB + 36864 + lr1 * 160 + wp * 8;

  auto load_w = [&](const char* sb, int grp) -> bf16x8 {
    const int kk = grp / 11, i = grp - kk * 11;
    const bool isq = (i == 10) || (WN == 1 && (i == 4 || i == 9));
    s16x4 lo, hi;
    if (isq) {
      const int qct = WN == 0 ? 0 : (i == 4 ? 1 : (i == 9 ? 2 : 3));
      lo = tr_read(sb + qoff0 + kk * 5120 + qct * 32);
      hi = tr_read(sb + qoff1 + kk * 5120 + qct * 32);
    } else {
      const int off = (i < 5 ? (WN * 5 + i) * 32 : 18432 + (WN * 5 + i - 5) * 32) + kk * 9216;
      lo = tr_read(sb + woff0 + off);
      hi = tr_read(sb + woff1 + off);
    }
    return __builtin_shufflevector(lo, hi, 0, 1, 2, 3, 4, 5, 6, 7);
  };

  const char* tbase;
  unsigned tvo;
  int tstep;
  {
    const int xcd = j >> 2, idx = (bm << 2) | (j & 3);
    if (w < 2) {
      const int li = l < 18 ? l : 17;
      tbase = (const char*)(w ? wu : wg); tstep = 64 * FF * 2;
      tvo = (unsigned)((idx * 2 + (li >= 9 ? 1 : 0)) * (FF * 2) + xcd * 1152 + (li >= 9 ? li - 9 : li) * 128);
    } else if (w == 2) {
      int cb = xcd * 512 + (l & 3) * 128;
      if (cb > NQKV * 2 - 128) cb = NQKV * 2 - 128;
      tbase = (const char*)wqkv; tstep = 64 * NQKV * 2;
      tvo = (unsigned)((idx * 2 + ((l >> 2) & 1)) * (NQKV * 2) + cb);
    } else {
      tbase = (const char*)x; tstep = 128;
      tvo = (unsigned)((idx * 64 + l) * (DM * 2));
    }
  }
  auto touch_step = [&](int st) {
    if (st > 8) st = 8;
    touch_line_s(tvo, tbase + (size_t)st * tstep, lds_base + 2 * G1_STAGE);
  };

  STAMPR(0, 14);
  STAMP(0, 0);
#pragma unroll
  for (int p = 0; p < 10; ++p) stage_piece(0, p);
  if (w < 4) { touch_step(1); touch_step(2); }
  STAMP(0, 12);
  int tl = 9;
  asm volatile("" : "+s"(tl));
#pragma clang loop unroll(disable)
  for (int t = 0; t < tl; ++t) {
    if (w < 4 && t < 8) asm volatile("s_waitcnt vmcnt(1)" ::: "memory");
    else asm volatile("s_waitcnt vmcnt(0)" ::: "memory");
    __builtin_amdgcn_s_barrier();
    asm volatile("" ::: "memory");
    STAMP(0, 1 + t);
    const bool issue = t < 8;
    const char* sb = lds + (t & 1) * G1_STAGE;
    bf16x8 xf[2][4], wfa, wfb, wfc;
    wfa = load_w(sb, 0);
#pragma unroll
    for (int rt = 0; rt < 4; ++rt) xf[0][rt] = *(const bf16x8*)(sb + xoff0 + rt * 2048);
    wfb = load_w(sb, 1);
    wfc = load_w(sb, 2);
#pragma unroll
    for (int grp = 0; grp < 22; ++grp) {
      const int kk = grp / 11, i = grp - kk * 11;
      bf16x8 wf = wfa;
      wfa = wfb;
      wfb = wfc;
      if (grp + 3 < 22) wfc = load_w(sb, grp + 3);
      if (grp >= 4 && grp < 8) xf[1][grp - 4] = *(const bf16x8*)(sb + xoff1 + (grp - 4) * 2048);
      if (grp < 10 && issue) stage_piece(t + 1, grp);
      if (grp == 10 && w < 4 && issue) touch_step(t + 3);
      const bool isq = (i == 10) || (WN == 1 && (i == 4 || i == 9));
#pragma unroll
      for (int rt = 0; rt < 4; ++rt) {
        if (ISV && isq) acc[i][rt] = MFMA16(xf[kk][rt], wf, acc[i][rt]);
        else acc[i][rt] = MFMA16(wf, xf[kk][rt], acc[i][rt]);
      }
      __builtin_amdgcn_sched_barrier(0);
    }
  }

  STAMP(0, 10);
  __builtin_amdgcn_sched_barrier(0);
  {
  const int l = lane_id_opaque();
  const int g = l >> 4;
  const int tok0 = bm * 256 + wm * 64;
  constexpr int NP = WN == 0 ? 5 : 4;
#pragma unroll
  for (int i = 0; i < NP; ++i) {
    const int ct = WN * 5 + i;
    const int chunk = j * 18 + ct * 2 + (g >> 1);
#pragma unroll
    for (int rt = 0; rt < 4; ++rt) {
      float hv[4];
#pragma unroll
      for (int r = 0; r < 4; ++r) hv[r] = bf16r(acc[i][rt][r]) * bf16r(acc[i + 5][rt][r]);
      u32x2 o = {pack_bf16(hv[0], hv[1]), pack_bf16(hv[2], hv[3])};
      int token = tok0 + rt * 16 + (l & 15);
      *(u32x2*)(hT + ((size_t)chunk * SEQ + token) * 8 + (g & 1) * 4) = o;
    }
  }
  if (j >= 28) {
    const int tid = w * 64 + l;
    const int token = bm * 256 + (j - 28) * 64 + (tid >> 3), prt = tid & 7;
    const u16* xr = x + (size_t)token * DM + prt * 72;
    const u16* gr = gw + prt * 72;
    bf16x8 xv[9], gv[9];
#pragma unroll
    for (int i = 0; i < 9; ++i) { xv[i] = *(const bf16x8*)(xr + i * 8); gv[i] = *(const bf16x8*)(gr + i * 8); }
    float sum = 0.f;
#pragma unroll
    for (int i = 0; i < 9; ++i)
#pragma unroll
      for (int e = 0; e < 8; ++e) sum += bf16r(bf2f((u16)xv[i][e]) * bf2f((u16)gv[i][e]));
    sum += __shfl_xor(sum, 1);
    sum += __shfl_xor(sum, 2);
    sum += __shfl_xor(sum, 4);
    sum = bf16r(sum);
    if (prt == 0) gout[token] = bf16r(1.0f / (1.0f + __expf(-sum)));
  }
  if (j < 27) {
    constexpr int NQ = WN == 0 ? 1 : 3;
#pragma unroll
    for (int qi = 0; qi < NQ; ++qi) {
      const int ai = WN == 0 ? 10 : (qi == 0 ? 4 : (qi == 1 ? 9 : 10));
      const int qct = WN == 0 ? 0 : qi + 1;
      if (ISV) {
        int cv = j * 64 + qct * 16 + (l & 15) - 1152;
        int head = cv / 18, d = cv - head * 18;
#pragma unroll
        for (int rt = 0; rt < 4; ++rt) {
          int t16 = tok0 + rt * 16;
          int tile32 = t16 >> 5, s = rt & 1;
          char* dst = KV + (size_t)(head * 64 + tile32) * KVREC + 1024 + ((s * 2 + (g & 1)) * 19 + d) * 16 + (g >> 1) * 8;
          u32x2 o = {pack_bf16(acc[ai][rt][0], acc[ai][rt][1]), pack_bf16(acc[ai][rt][2], acc[ai][rt][3])};
          *(u32x2*)dst = o;
          if (d == 17) *(u32x2*)(dst + 16) = (u32x2){0x3F803F80u, 0x3F803F80u};
        }
      } else {
        const bool isQ = j < 9;
        const float sc = isQ ? 0.23570226039551584f * 1.4426950408889634f : 1.0f;
#pragma unroll
        for (int pr = 0; pr < 2; ++pr) {
          int c = j * 64 + qct * 16 + 4 * g + 2 * pr - (isQ ? 0 : 576);
          int head = c / 18, d = c - head * 18;
#pragma unroll
          for (int rt = 0; rt < 4; ++rt) {
            int token = tok0 + rt * 16 + (l & 15);
            unsigned v = pack_bf16(acc[ai][rt][2 * pr] * sc, acc[ai][rt][2 * pr + 1] * sc);
            char* dst;
            if (isQ) dst = (char*)(Qb + ((size_t)head * SEQ + token) * 32 + d);
            else {
              char* rec = KV + (size_t)(head * 64 + (token >> 5)) * KVREC;
              dst = d < 16 ? rec + (token & 31) * 32 + d * 2 : rec + 2240 + (token & 31) * 4;
            }
            *(unsigned*)dst = v;
          }
        }
      }
    }
  }
  }
  STAMP(0, 11);
  STAMPR(0, 15);
}

__global__ __launch_bounds__(512, 2) void gemm1_kernel(const u16* __restrict__ x, const u16* __restrict__ wg,
                                                       const u16* __restrict__ wu, const u16* __restrict__ wqkv,
                                                       u16* __restrict__ hT, u16* __restrict__ Qb,
                                                       char* __restrict__ KV, const u16* __restrict__ gw,
                                                       float* __restrict__ gout) {
  __shared__ __attribute__((aligned(16))) char lds[2 * G1_STAGE + 256];
  const int b = blockIdx.x;
  const int xcd = b & 7, idx = b >> 3;
  const int j = xcd * 4 + (idx & 3), bm = idx >> 2;
  const int w = __builtin_amdgcn_readfirstlane(threadIdx.x >> 6);
  const bool isv = (j >= 18);
  if (w & 1) {
    if (isv) gemm1_body<true, 1>(x, wg, wu, wqkv, hT, Qb, KV, gw, gout, lds, bm, j, w);
    else gemm1_body<false, 1>(x, wg, wu, wqkv, hT, Qb, KV, gw, gout, lds, bm, j, w);
  } else {
    if (isv) gemm1_body<true, 0>(x, wg, wu, wqkv, hT, Qb, KV, gw, gout, lds, bm, j, w);
    else gemm1_body<false, 0>(x, wg, wu, wqkv, hT, Qb, KV, gw, gout, lds, bm, j, w);
  }
}

DI float xhalf_max(float v) {
  auto rr = __builtin_amdgcn_permlane32_swap(__float_as_uint(v), __float_as_uint(v), false, false);
  return fmaxf(__uint_as_float(rr[0]), __uint_as_float(rr[1]));
}
DI float tile_max(const f32x16& S) {
  float t = fmaxf(fmaxf(S[0], S[1]), S[2]);
#pragma unroll
  for (int i = 3; i < 15; i += 2) t = fmaxf(fmaxf(t, S[i]), S[i + 1]);
  return xhalf_max(fmaxf(t, S[15]));
}
DI f32x16 qk_tile(bf16x8 kA, unsigned kH, bf16x8 qA, u32x4 qB) {
  f32x16 S;
#pragma unroll
  for (int i = 0; i < 16; ++i) S[i] = 0.f;
  u32x4 kBv = {kH, 0x00003F80u, 0u, 0u};
  S = MFMA32(kA, qA, S);
  S = MFMA32(__builtin_bit_cast(bf16x8, kBv), __builtin_bit_cast(bf16x8, qB), S);
  return S;
}
DI void pv_tile(f32x16& S, bf16x8 v0, bf16x8 v1, f32x16& O) {
#pragma unroll
  for (int i = 0; i < 16; ++i) S[i] = __builtin_amdgcn_exp2f(S[i]);
  u32x4 p0 = {pack_bf16(S[0], S[1]), pack_bf16(S[2], S[3]), pack_bf16(S[4], S[5]), pack_bf16(S[6], S[7])};
  u32x4 p1 = {pack_bf16(S[8], S[9]), pack_bf16(S[10], S[11]), pack_bf16(S[12], S[13]), pack_bf16(S[14], S[15])};
  O = MFMA32(v0, __builtin_bit_cast(bf16x8, p0), O);
  O = MFMA32(v1, __builtin_bit_cast(bf16x8, p1), O);
}

constexpr int AT_STAGE = 2 * 8 * KVREC;

DI void attn_body(const u16* __restrict__ Qb, const char* __restrict__ KV, u16* __restrict__ oT, char* lds,
                  const int b) {
  const int head = (b & 7) * 4 + ((b >> 3) & 3), qblk = b >> 5;
  const int w = __builtin_amdgcn_readfirstlane(threadIdx.x >> 6);
  const int l = threadIdx.x & 63, r = l & 31, h = l >> 5;
  const int qp = w & 3, kvh = w >> 2;
  const int token0 = qblk * 256 + qp * 64 + r;

  const unsigned lds_base = (unsigned)(size_t)(LDS_AS char*)lds;
  const char* kvsrc = KV + (size_t)(head * 64) * KVREC;
  auto stage = [&](int s) {
#pragma unroll
    for (int i = 0; i < 5; ++i) {
      int n = w * 5 + i;
      if (n > 37) n -= 2;
      const int hf = n >= 19 ? 1 : 0, pc = n - hf * 19;
      glds16_asm((unsigned)(l * 16), kvsrc + (hf * 32 + s * 8) * KVREC + pc * 1024,
                 lds_base + (s & 1) * AT_STAGE + hf * (8 * KVREC) + pc * 1024);
    }
  };
  STAMPR(1, 14);
  STAMP(1, 0);
  stage(0);

  bf16x8 qA[2];
  u32x4 qB[2];
  float m[2];
  f32x16 O[2];
#pragma unroll
  for (int q = 0; q < 2; ++q) {
    const u16* qrow = Qb + ((size_t)head * SEQ + token0 + q * 32) * 32;
    qA[q] = *(const bf16x8*)(qrow + 8 * h);
    unsigned q67 = *(const unsigned*)(qrow + 16);
    asm volatile("" : "+v"(qA[q]), "+v"(q67));
    qB[q] = (u32x4){h ? 0u : q67, 0u, 0u, 0u};
    m[q] = 0.f;
#pragma unroll
    for (int i = 0; i < 16; ++i) O[q][i] = 0.f;
  }
  bool failed = false;

  const int koff = kvh * (8 * KVREC) + r * 32 + h * 16;
  const int hoff = kvh * (8 * KVREC) + 2240 + r * 4;
  const int voff = kvh * (8 * KVREC) + 1024 + (h * 19 + (r < 18 ? r : 18)) * 16;

#pragma clang loop unroll(disable)
  for (int s = 0; s < 4; ++s) {
    asm volatile("s_waitcnt vmcnt(0)" ::: "memory");
    __builtin_amdgcn_s_barrier();
    asm volatile("" ::: "memory");
    STAMP(1, 1 + s);
    if (s < 3) stage(s + 1);
    const char* sb = lds + (s & 1) * AT_STAGE;
    bf16x8 kA = *(const bf16x8*)(sb + koff);
    unsigned kH = *(const unsigned*)(sb + hoff);
    if (s == 0) {
#pragma unroll
      for (int q = 0; q < 2; ++q) {
        f32x16 S0 = qk_tile(kA, kH, qA[q], qB[q]);
        m[q] = bf16r(tile_max(S0));
        qB[q][1] = h ? 0u : pack_bf16(-m[q], 0.f);
      }
    }
    f32x16 Sa = qk_tile(kA, kH, qA[0], qB[0]);
#pragma unroll
    for (int i = 0; i < 8; ++i) {
      bf16x8 v0 = *(const bf16x8*)(sb + voff + i * KVREC);
      bf16x8 v1 = *(const bf16x8*)(sb + voff + i * KVREC + 608);
      f32x16 Sb = qk_tile(kA, kH, qA[1], qB[1]);
      if (i < 7) {
        kA = *(const bf16x8*)(sb + koff + (i + 1) * KVREC);
        kH = *(const unsigned*)(sb + hoff + (i + 1) * KVREC);
      }
      pv_tile(Sa, v0, v1, O[0]);
      if (i < 7) Sa = qk_tile(kA, kH, qA[0], qB[0]);
      pv_tile(Sb, v0, v1, O[1]);
    }
    float lmax = h ? 0.f : fmaxf(O[0][10], O[1][10]);
    if (__any(!(lmax < 4294967296.f) || !(O[0][10] == O[0][10]) || !(O[1][10] == O[1][10]))) {
#pragma unroll
      for (int q = 0; q < 2; ++q) {
        float lv = O[q][10];
        auto rr = __builtin_amdgcn_permlane32_swap(__float_as_uint(lv), __float_as_uint(lv), false, false);
        lv = __uint_as_float(rr[0]);
        if (__any(!(lv < 3.0e38f))) failed = true;
        float sh = lv > 1.f && lv < 3.0e38f ? floorf(__builtin_amdgcn_logf(lv)) : 0.f;
        float mn = bf16r(m[q] + sh);
        float sc = __builtin_amdgcn_exp2f(m[q] - mn);
        m[q] = mn;
#pragma unroll
        for (int i = 0; i < 16; ++i) O[q][i] *= sc;
        qB[q][1] = h ? 0u : pack_bf16(-mn, 0.f);
      }
    }
  }

  STAMP(1, 5);
  if (failed) {
#pragma clang loop unroll(disable)
    for (int q = 0; q < 2; ++q) {
      const char* rec = KV + (size_t)(head * 64 + kvh * 32) * KVREC;
      const bf16x8 qa = q ? qA[1] : qA[0];
      u32x4 qb = q ? qB[1] : qB[0];
      qb[1] = 0u;
      float mm = -3.0e38f;
      f32x16 Oo;
#pragma unroll
      for (int i = 0; i < 16; ++i) Oo[i] = 0.f;
#pragma clang loop unroll(disable)
      for (int it = 0; it < 32; ++it, rec += KVREC) {
        bf16x8 kA = *(const bf16x8*)(rec + r * 32 + h * 16);
        unsigned kH = *(const unsigned*)(rec + 2240 + r * 4);
        bf16x8 v0 = *(const bf16x8*)(rec + 1024 + (h * 19 + (r < 18 ? r : 18)) * 16);
        bf16x8 v1 = *(const bf16x8*)(rec + 1024 + 608 + (h * 19 + (r < 18 ? r : 18)) * 16);
        f32x16 S = qk_tile(kA, kH, qa, qb);
        float mn = fmaxf(mm, tile_max(S));
        float sc = __builtin_amdgcn_exp2f(mm - mn);
        mm = mn;
#pragma unroll
        for (int i = 0; i < 16; ++i) { S[i] -= mn; Oo[i] *= sc; }
        pv_tile(S, v0, v1, Oo);
      }
      if (q) { O[1] = Oo; m[1] = mm; } else { O[0] = Oo; m[0] = mm; }
    }
  }

  __syncthreads();
  float* mrg = (float*)lds;
  if (kvh == 1) {
#pragma unroll
    for (int q = 0; q < 2; ++q) {
#pragma unroll
      for (int i = 0; i < 12; ++i) mrg[((qp * 2 + q) * 13 + i) * 64 + l] = O[q][i];
      mrg[((qp * 2 + q) * 13 + 12) * 64 + l] = m[q];
    }
  }
  __syncthreads();
  if (kvh == 0) {
#pragma unroll
    for (int q = 0; q < 2; ++q) {
      float m1 = mrg[((qp * 2 + q) * 13 + 12) * 64 + l];
      float mm = fmaxf(m[q], m1);
      float a0 = __builtin_amdgcn_exp2f(m[q] - mm), a1 = __builtin_amdgcn_exp2f(m1 - mm);
      float o[12];
#pragma unroll
      for (int i = 0; i < 12; ++i) o[i] = O[q][i] * a0 + mrg[((qp * 2 + q) * 13 + i) * 64 + l] * a1;
      float lsum = __shfl(o[10], r);
      float inv = 1.0f / lsum;
      const int token = token0 + q * 32;
#pragma unroll
      for (int pr = 0; pr < 5; ++pr) {
        if (pr == 4 && h) continue;
        int d = (pr >> 1) * 8 + (pr & 1) * 2 + 4 * h;
        int c = head * 18 + d;
        unsigned v = pack_bf16(o[2 * pr] * inv, o[2 * pr + 1] * inv);
        *(unsigned*)(oT + ((size_t)(c >> 3) * SEQ + token) * 8 + (c & 7)) = v;
      }
    }
  }
  STAMP(1, 6);
  STAMPR(1, 15);
}

template <int N> DI void wait_vm() { asm volatile("s_waitcnt vmcnt(%0)" ::"i"(N) : "memory"); }
DI void raw_barrier() {
  __builtin_amdgcn_s_barrier();
  asm volatile("" ::: "memory");
}

template <int NW, int RT, int NS = 4>
struct Gemm2 {
  static constexpr int BM = NW * RT * 16;
  static constexpr int AB = 8 * BM * 16;
  static constexpr int STG = AB + 18432;
  static constexpr int NAW = AB / 1024 / NW;
  static constexpr int NWW = (18 + NW - 1) / NW;
  static constexpr int NV = NAW + NWW;
  static constexpr int LA = 4;

  const char* abase;
  const char* wbase;
  unsigned avo[NAW], wvo[NWW];
  unsigned lds_base;
  char* lds;
  int w, aoff, woff0, woff1;
  bf16x8 af[2][RT], wq_[LA];

  DI void init(const u16* AT, const u16* W, int tok0, int col0, int kbeg, char* lds_, int w_, int l) {
    lds = lds_; w = w_;
    lds_base = (unsigned)(size_t)(LDS_AS char*)lds_;
    abase = (const char*)(AT + (size_t)(kbeg / 8) * SEQ * 8);
    wbase = (const char*)(W + (size_t)kbeg * DM);
    const int g = l >> 4;
#pragma unroll
    for (int i = 0; i < NAW; ++i) {
      int off = (w * NAW + i) * 64 + l;
      int chunk = off / BM, token = off - chunk * BM;
      avo[i] = (unsigned)((chunk * SEQ + tok0 + token) * 16);
    }
#pragma unroll
    for (int i = 0; i < NWW; ++i) {
      int n = w + NW * i;
      if (n >= 18) n = 16 + (n & 1);
      int c = n * 64 + l;
      int lr = c / 18, ch = c - lr * 18;
      int krow = lr ^ ((lr & 8) >> 1);
      wvo[i] = (unsigned)((krow * DM + col0 + ch * 8) * 2);
    }
    aoff = g * (BM * 16) + (w * RT * 16 + (l & 15)) * 16;
    const int wq = (l & 15) >> 2, wp = l & 3;
    woff0 = AB + (g * 8 + 4 * (0 ^ (g & 1)) + wq) * 288 + wp * 8;
    woff1 = AB + (g * 8 + 4 * (1 ^ (g & 1)) + wq) * 288 + wp * 8;
  }
  DI void piece(int t, int p) {
    const unsigned sb = lds_base + (t & (NS - 1)) * STG;
    if (p < NAW) {
      glds16_asm(avo[p], abase + (size_t)t * (8 * SEQ * 16), sb + (w * NAW + p) * 1024);
    } else {
      const int i = p - NAW;
      int n = w + NW * i;
      if (n >= 18) n = 16 + (n & 1);
      glds16_asm(wvo[i], wbase + (size_t)t * (64 * DM * 2), sb + AB + n * 1024);
    }
  }
  DI bf16x8 load_w(const char* sb, int grp) {
    const int kk = grp / 9, ct = grp - kk * 9;
    s16x4 lo = tr_read(sb + woff0 + kk * 9216 + ct * 32);
    s16x4 hi = tr_read(sb + woff1 + kk * 9216 + ct * 32);
    return __builtin_shufflevector(lo, hi, 0, 1, 2, 3, 4, 5, 6, 7);
  }
  DI bf16x8 load_a(const char* sb, int kk, int rt) {
    return *(const bf16x8*)(sb + aoff + kk * 4 * BM * 16 + rt * 256);
  }
  DI void prologue_issue() {
#pragma unroll
    for (int t = 0; t < 3; ++t)
#pragma unroll
      for (int p = 0; p < NV; ++p) piece(t, p);
  }
  DI void prologue_read() {
    wait_vm<2 * NV>();
    raw_barrier();
#pragma unroll
    for (int kk = 0; kk < 2; ++kk)
#pragma unroll
      for (int rt = 0; rt < RT; ++rt) af[kk][rt] = load_a(lds, kk, rt);
#pragma unroll
    for (int i = 0; i < LA; ++i) wq_[i] = load_w(lds, i);
  }
  template <bool ISSUE, bool PREF>
  DI void body(int t, f32x4 (&acc)[9][RT]) {
    const char* sb = lds + (t & 3) * STG;
    const char* sn = lds + ((t + 1) & 3) * STG;
    bf16x8 an[2][RT];
#pragma unroll
    for (int grp = 0; grp < 18; ++grp) {
      bf16x8 wf = wq_[0];
#pragma unroll
      for (int i = 0; i + 1 < LA; ++i) wq_[i] = wq_[i + 1];
      if (grp + LA < 18) wq_[LA - 1] = load_w(sb, grp + LA);
      else if (PREF) wq_[LA - 1] = load_w(sn, grp + LA - 18);
      if (PREF && grp >= 8 && grp < 8 + 2 * RT) an[(grp - 8) / RT][(grp - 8) % RT] = load_a(sn, (grp - 8) / RT, (grp - 8) % RT);
      if (ISSUE && grp < NV) piece(t + 3, grp);
      const int kk = grp / 9, ct = grp - kk * 9;
#pragma unroll
      for (int rt = 0; rt < RT; ++rt) acc[ct][rt] = MFMA16(wf, af[kk][rt], acc[ct][rt]);
      __builtin_amdgcn_sched_barrier(0);
    }
    if (PREF) {
#pragma unroll
      for (int kk = 0; kk < 2; ++kk)
#pragma unroll
        for (int rt = 0; rt < RT; ++rt) af[kk][rt] = an[kk][rt];
    }
  }
  template <bool ISSUE>
  DI void body2(int t, f32x4 (&acc)[9][RT]) {
    const char* sb = lds + (t & 1) * STG;
#pragma unroll
    for (int kk = 0; kk < 2; ++kk)
#pragma unroll
      for (int rt = 0; rt < RT; ++rt) af[kk][rt] = load_a(sb, kk, rt);
#pragma unroll
    for (int i = 0; i < LA; ++i) wq_[i] = load_w(sb, i);
#pragma unroll
    for (int grp = 0; grp < 18; ++grp) {
      bf16x8 wf = wq_[0];
#pragma unroll
      for (int i = 0; i + 1 < LA; ++i) wq_[i] = wq_[i + 1];
      if (grp + LA < 18) wq_[LA - 1] = load_w(sb, grp + LA);
      if (ISSUE && grp < NV) piece(t + 1, grp);
      const int kk = grp / 9, ct = grp - kk * 9;
#pragma unroll
      for (int rt = 0; rt < RT; ++rt) acc[ct][rt] = MFMA16(wf, af[kk][rt], acc[ct][rt]);
      __builtin_amdgcn_sched_barrier(0);
    }
  }
  DI void run2(int nsteps, f32x4 (&acc)[9][RT]) {
    int tl = nsteps - 1;
    asm volatile("" : "+s"(tl));
#pragma clang loop unroll(disable)
    for (int t = 0; t < tl; ++t) {
      wait_vm<0>();
      raw_barrier();
      body2<true>(t, acc);
    }
    wait_vm<0>();
    raw_barrier();
    body2<false>(tl, acc);
  }
  DI void run(int nsteps, f32x4 (&acc)[9][RT]) {
    prologue_read();
    int tl = nsteps - 3;
    asm volatile("" : "+s"(tl));
#pragma clang loop unroll(disable)
    for (int t = 0; t < tl; ++t) {
      wait_vm<NV>();
      raw_barrier();
      body<true, true>(t, acc);
    }
    wait_vm<NV>();
    raw_barrier();
    body<false, true>(tl, acc);
    wait_vm<0>();
    raw_barrier();
    body<false, true>(tl + 1, acc);
    body<false, false>(tl + 2, acc);
  }
};

DI void down_body(const u16* __restrict__ hT, const u16* __restrict__ wd, u16* __restrict__ part, char* lds,
                  const int b) {
  using G = Gemm2<8, 1, 2>;
  const int xcd = b & 7, idx = b >> 3;
  const int ks = xcd >> 1;
  const int bm = (xcd & 1) * 8 + (idx >> 2);
  const int bn = idx & 3;
  const int w = __builtin_amdgcn_readfirstlane(threadIdx.x >> 6);
  const int l = threadIdx.x & 63;
  STAMPR(2, 14);
  STAMP(2, 0);
  G gm;
  gm.init(hT, wd, bm * 128, bn * 144, ks * 1152, lds, w, l);
#pragma unroll
  for (int p = 0; p < G::NV; ++p) gm.piece(0, p);
  {
    const int Gi = idx * 512 + threadIdx.x;
    const unsigned scr = gm.lds_base + 2 * G::STG;
    const char* hb = (const char*)hT + ((size_t)(ks * 144) * SEQ + (xcd & 1) * 1024) * 16;
    touch_line(hb + (size_t)(Gi >> 7) * (SEQ * 16) + (Gi & 127) * 128, scr);
    const int G2 = 16384 + (Gi & 2047);
    touch_line(hb + (size_t)(G2 >> 7) * (SEQ * 16) + (G2 & 127) * 128, scr);
    const int G3 = Gi < 10368 ? Gi : 10367;
    touch_line((const char*)wd + (size_t)ks * (1152 * DM * 2) + (size_t)G3 * 128, scr);
  }
  STAMP(2, 1);
  f32x4 acc[9][1];
#pragma unroll
  for (int i = 0; i < 9; ++i) acc[i][0] = (f32x4){0.f, 0.f, 0.f, 0.f};
  gm.run2(18, acc);
  STAMP(2, 2);
#pragma unroll
  for (int ct = 0; ct < 9; ++ct) {
    int tt = bm * 8 + w, ctg = bn * 9 + ct;
    u32x2 pv = {pack_bf16(acc[ct][0][0], acc[ct][0][1]), pack_bf16(acc[ct][0][2], acc[ct][0][3])};
    *(u32x2*)(part + ((size_t)(ks * 128 + tt) * 36 + ctg) * 256 + l * 4) = pv;
  }
  STAMP(2, 3);
  STAMPR(2, 15);
}

__global__ __launch_bounds__(512, 4) void mid_kernel(const u16* __restrict__ Qb, const char* __restrict__ KV,
                                                     u16* __restrict__ oT, const u16* __restrict__ hT,
                                                     const u16* __restrict__ wd, u16* __restrict__ part) {
  __shared__ __attribute__((aligned(16))) char lds[2 * AT_STAGE];
  const int b = blockIdx.x;
  if (b >= 256) down_body(hT, wd, part, lds, b - 256);
  else attn_body(Qb, KV, oT, lds, b);
}

__global__ __launch_bounds__(256, 2) void final_kernel(const u16* __restrict__ oT, const u16* __restrict__ wo,
                                                       const u16* __restrict__ part, const float* __restrict__ gin,
                                                       u16* __restrict__ out) {
  __shared__ __attribute__((aligned(16))) char lds[576 * 96 + 2048];
  const int b = blockIdx.x;
  const int bm = b / 12, bn = b - bm * 12;
  const int w = __builtin_amdgcn_readfirstlane(threadIdx.x >> 6);
  const int l = threadIdx.x & 63, g = l >> 4;
  STAMPR(3, 14);
  STAMP(3, 0);
  const unsigned lds_base = (unsigned)(size_t)(LDS_AS char*)lds;
#pragma unroll
  for (int i = 0; i < 14; ++i) {
    const int n = w * 14 + i;
    int c = n * 64 + l;
    int lr = c / 6, ch = c - lr * 6;
    if (lr > 575) lr = 575;
    int krow = lr ^ ((lr & 8) >> 1);
    glds16_asm((unsigned)((krow * DM + bn * 48 + ch * 8) * 2), wo, lds_base + n * 1024);
  }
  const int token = bm * 64 + w * 16 + (l & 15);
  bf16x8 af[18];
#pragma unroll
  for (int ks = 0; ks < 18; ++ks) af[ks] = *(const bf16x8*)(oT + ((size_t)(ks * 4 + g) * SEQ + token) * 8);
  float gg = gin[token];
  const int tt = bm * 4 + w;
  f32x4 bsum[3];
#pragma unroll
  for (int ct = 0; ct < 3; ++ct) {
    int ctg = bn * 3 + ct;
    f32x4 pp[4];
#pragma unroll
    for (int ks = 0; ks < 4; ++ks) {
      u32x2 v = *(const u32x2*)(part + ((size_t)(ks * 128 + tt) * 36 + ctg) * 256 + l * 4);
      pp[ks] = (f32x4){__uint_as_float(v[0] << 16), __uint_as_float(v[0] & 0xffff0000u),
                       __uint_as_float(v[1] << 16), __uint_as_float(v[1] & 0xffff0000u)};
    }
    bsum[ct] = (pp[0] + pp[1]) + (pp[2] + pp[3]);
  }
  STAMP(3, 1);
  asm volatile("s_waitcnt vmcnt(0)" ::: "memory");
  __builtin_amdgcn_s_barrier();
  asm volatile("" ::: "memory");
  STAMP(3, 2);
  const int wq = (l & 15) >> 2, wp = l & 3;
  const int woff0 = (g * 8 + 4 * (0 ^ (g & 1)) + wq) * 96 + wp * 8;
  const int woff1 = (g * 8 + 4 * (1 ^ (g & 1)) + wq) * 96 + wp * 8;
  f32x4 acc[3];
#pragma unroll
  for (int i = 0; i < 3; ++i) acc[i] = (f32x4){0.f, 0.f, 0.f, 0.f};
#pragma unroll
  for (int ks = 0; ks < 18; ++ks)
#pragma unroll
    for (int ct = 0; ct < 3; ++ct) {
      s16x4 lo = tr_read(lds + woff0 + ks * 3072 + ct * 32);
      s16x4 hi = tr_read(lds + woff1 + ks * 3072 + ct * 32);
      bf16x8 wf = __builtin_shufflevector(lo, hi, 0, 1, 2, 3, 4, 5, 6, 7);
      acc[ct] = MFMA16(wf, af[ks], acc[ct]);
    }
  STAMP(3, 3);
  const float og = bf16r(1.0f - gg);
#pragma unroll
  for (int ct = 0; ct < 3; ++ct) {
    int ctg = bn * 3 + ct;
    float o[4];
#pragma unroll
    for (int e = 0; e < 4; ++e) o[e] = bf16r(gg * bf16r(acc[ct][e])) + bf16r(og * bf16r(bsum[ct][e]));
    u32x2 ov = {pack_bf16(o[0], o[1]), pack_bf16(o[2], o[3])};
    *(u32x2*)(out + (size_t)token * DM + ctg * 16 + g * 4) = ov;
  }
  STAMP(3, 4);
  STAMPR(3, 15);
}

extern "C" void kernel_launch(void* const* d_in, const int* in_sizes, int n_in, void* d_out, int out_size,
                              void* d_ws, size_t ws_size, hipStream_t stream) {
  const u16* x = (const u16*)d_in[0];
  const u16* wqkv = (const u16*)d_in[1];
  const u16* wo = (const u16*)d_in[2];
  const u16* wg = (const u16*)d_in[4];
  const u16* wu = (const u16*)d_in[5];
  const u16* wd = (const u16*)d_in[6];
  const u16* gw = (const u16*)d_in[7];
  char* ws = (char*)d_ws;
  u16* hT = (u16*)(ws + WS_HT);
  u16* Qb = (u16*)(ws + WS_QB);
  char* KV = ws + WS_KV;
  u16* oT = (u16*)(ws + WS_OT);
  u16* part = (u16*)(ws + WS_PART);
  float* gbuf = (float*)(ws + WS_G);
  u16* out = (u16*)d_out;

  gemm1_kernel<<<256, 512, 0, stream>>>(x, wg, wu, wqkv, hT, Qb, KV, gw, gbuf);
  mid_kernel<<<512, 512, 0, stream>>>(Qb, KV, oT, hT, wd, part);
  final_kernel<<<384, 256, 0, stream>>>(oT, wo, part, gbuf, out);
}
```

```cpp
#include <hip/hip_runtime.h>
#include <hip/hip_bf16.h>
#include <stdint.h>
#include <type_traits>

typedef unsigned short u16;
typedef __attribute__((ext_vector_type(8))) short bf16x8;
typedef __attribute__((ext_vector_type(4))) short s16x4;
typedef __attribute__((ext_vector_type(4))) float f32x4;
typedef __attribute__((ext_vector_type(16))) float f32x16;
typedef __attribute__((ext_vector_type(2))) float f32x2;
typedef __attribute__((ext_vector_type(2))) __bf16 bf16x2_t;
typedef __attribute__((ext_vector_type(2))) unsigned u32x2;
typedef __attribute__((ext_vector_type(4))) unsigned u32x4;

#define DI __device__ __forceinline__
#define LDS_AS __attribute__((address_space(3)))

constexpr int SEQ = 2048, DM = 576, FF = 4608, NH = 32, DH = 18, NQKV = 1728;

DI unsigned pack_bf16(float lo, float hi) {
  f32x2 f = {lo, hi};
  bf16x2_t b = __builtin_convertvector(f, bf16x2_t);
  return __builtin_bit_cast(unsigned, b);
}
DI float bf16r(float x) { return __uint_as_float(pack_bf16(x, x) << 16); }
DI float bf2f(u16 v) { return __uint_as_float(((unsigned)v) << 16); }

DI s16x4 tr_read(const char* lds_ptr) {
  return __builtin_amdgcn_ds_read_tr16_b64_v4i16((LDS_AS s16x4*)(lds_ptr));
}
DI void glds16(const void* gsrc, char* lds_dst) {
  __builtin_amdgcn_global_load_lds((const __attribute__((address_space(1))) void*)gsrc,
                                   (LDS_AS void*)lds_dst, 16, 0, 0);
}
DI int lane_id_opaque() {
  int l;
  asm volatile("v_mbcnt_lo_u32_b32 %0, -1, 0\n\tv_mbcnt_hi_u32_b32 %0, -1, %0" : "=v"(l));
  return l;
}
DI void glds16_asm(unsigned voff, const void* sbase, unsigned lds_dst) {
  unsigned keep;
  asm volatile("s_mov_b32 %0, m0\n\ts_mov_b32 m0, %3\n\ts_nop 0\n\tglobal_load_lds_dwordx4 %1, %2\n\ts_mov_b32 m0, %0"
               : "=&s"(keep) : "v"(voff), "s"(sbase), "s"(lds_dst) : "memory");
}
DI void touch_line(const void* gptr, unsigned lds_scratch) {
  unsigned keep;
  asm volatile("s_mov_b32 %0, m0\n\ts_mov_b32 m0, %2\n\ts_nop 0\n\tglobal_load_lds_dword %1, off\n\ts_mov_b32 m0, %0"
               : "=&s"(keep) : "v"(gptr), "s"(lds_scratch) : "memory");
}
DI void touch_line_s(unsigned voff, const void* sbase, unsigned lds_scratch) {
  unsigned keep;
  asm volatile("s_mov_b32 %0, m0\n\ts_mov_b32 m0, %3\n\ts_nop 0\n\tglobal_load_lds_dword %1, %2\n\ts_mov_b32 m0, %0"
               : "=&s"(keep) : "v"(voff), "s"(sbase), "s"(lds_scratch) : "memory");
}
#define STAMP(kid, i) do {} while (0)
#define STAMPR(kid, i) do {} while (0)
#define MFMA16(a, b, c) __builtin_amdgcn_mfma_f32_16x16x32_bf16((a), (b), (c), 0, 0, 0)
#define MFMA32(a, b, c) __builtin_amdgcn_mfma_f32_32x32x16_bf16((a), (b), (c), 0, 0, 0)

constexpr size_t WS_HT = 0;
constexpr size_t WS_QB = WS_HT + (size_t)FF * SEQ * 2;
constexpr int KVREC = 2432;
constexpr size_t WS_KV = WS_QB + (size_t)NH * SEQ * 32 * 2;
constexpr size_t WS_VF = WS_KV;
constexpr size_t WS_OT = WS_KV + (size_t)NH * 64 * KVREC;
constexpr size_t WS_PART = WS_OT + (size_t)DM * SEQ * 2;
constexpr size_t WS_G = WS_PART + (size_t)4 * SEQ * DM * 4;
constexpr size_t WS_END = WS_G + SEQ * 4;

constexpr int G1_XB = 256 * 128;
constexpr int G1_WB = 18432 * 2 + 10240;
constexpr int G1_STAGE = G1_XB + G1_WB;

template <bool ISV, int WN>
DI void gemm1_body(const u16* __restrict__ x, const u16* __restrict__ wg, const u16* __restrict__ wu,
                   const u16* __restrict__ wqkv, u16* __restrict__ hT, u16* __restrict__ Qb,
                   char* __restrict__ KV, const u16* __restrict__ gw, float* __restrict__ gout, char* lds,
                   int bm, int j, int w) {
  const int l = lane_id_opaque();
  const int wm = w >> 1;
  const int g = l >> 4;
  const int jq = j < 27 ? j : 26;

  unsigned xvo, guvo[2], evo[2];
  const char* ebase[2];
  int estep, edst[2];
  {
    int row = 32 * w + (l >> 3);
    int chunk = (l & 7) ^ (l >> 3);
    xvo = (unsigned)((bm * 256 + row) * DM + chunk * 8) * 2u;
#pragma unroll
    for (int i = 0; i < 2; ++i) {
      int c = (w + 8 * i) * 64 + l;
      int lr = c / 18, ch = c - lr * 18;
      int krow = lr ^ ((lr & 8) >> 1);
      guvo[i] = (unsigned)(krow * FF + j * 144 + ch * 8) * 2u;
    }
    if (w < 2) {
      int c = (16 + w) * 64 + l;
      int lr = c / 18, ch = c - lr * 18;
      int krow = lr ^ ((lr & 8) >> 1);
      evo[0] = evo[1] = (unsigned)(krow * FF + j * 144 + ch * 8) * 2u;
      ebase[0] = (const char*)wg; ebase[1] = (const char*)wu;
      estep = 64 * FF * 2;
      edst[0] = G1_XB + (16 + w) * 1024; edst[1] = G1_XB + 18432 + (16 + w) * 1024;
    } else {
#pragma unroll
      for (int i = 0; i < 2; ++i) {
        int n = (w - 2) + 6 * i;
        if (n > 9) n = 9;
        int c = n * 64 + l;
        int lr = c / 10, ch = c - lr * 10;
        if (ch > 7) ch = 7;
        int krow = lr ^ ((lr & 8) >> 1);
        evo[i] = (unsigned)(krow * NQKV + jq * 64 + ch * 8) * 2u;
        ebase[i] = (const char*)wqkv;
        edst[i] = G1_XB + 36864 + n * 1024;
      }
      estep = 64 * NQKV * 2;
    }
    edst[0] = __builtin_amdgcn_readfirstlane(edst[0]);
    edst[1] = __builtin_amdgcn_readfirstlane(edst[1]);
  }

  const unsigned lds_base = (unsigned)(size_t)(LDS_AS char*)lds;
  auto stage_piece = [&](int t, int p) {
    const unsigned sb = lds_base + (t & 1) * G1_STAGE;
    if (p < 4) {
      const char* xb = (const char*)x + t * 128 + p * 8 * DM * 2;
      glds16_asm(xvo, xb, sb + (32 * w + 8 * p) * 128);
    } else if (p < 8) {
      const int i = (p - 4) >> 1;
      const char* gb = (const char*)((p & 1) ? wu : wg) + (size_t)t * (64 * FF * 2);
      glds16_asm(guvo[i], gb, sb + G1_XB + (p & 1) * 18432 + (w + 8 * i) * 1024);
    } else {
      glds16_asm(evo[p & 1], ebase[p & 1] + (size_t)t * estep, sb + edst[p & 1]);
    }
  };

  f32x4 acc[11][4];
#pragma unroll
  for (int i = 0; i < 11; ++i)
#pragma unroll
    for (int r = 0; r < 4; ++r) acc[i][r] = (f32x4){0.f, 0.f, 0.f, 0.f};

  const int xoff0 = wm * 8192 + (l & 15) * 128 + (((0 + g) ^ (l & 7)) << 4);
  const int xoff1 = wm * 8192 + (l & 15) * 128 + (((4 + g) ^ (l & 7)) << 4);
  const int wq = (l & 15) >> 2, wp = l & 3;
  const int lr0 = g * 8 + 4 * (0 ^ (g & 1)) + wq, lr1 = g * 8 + 4 * (1 ^ (g & 1)) + wq;
  const int woff0 = G1_XB + lr0 * 288 + wp * 8, woff1 = G1_XB + lr1 * 288 + wp * 8;
  const int qoff0 = G1_XB + 36864 + lr0 * 160 + wp * 8, qoff1 = G1_XB + 36864 + lr1 * 160 + wp * 8;

  auto load_w = [&](const char* sb, int grp) -> bf16x8 {
    const int kk = grp / 11, i = grp - kk * 11;
    const bool isq = (i == 10) || (WN == 1 && (i == 4 || i == 9));
    s16x4 lo, hi;
    if (isq) {
      const int qct = WN == 0 ? 0 : (i == 4 ? 1 : (i == 9 ? 2 : 3));
      lo = tr_read(sb + qoff0 + kk * 5120 + qct * 32);
      hi = tr_read(sb + qoff1 + kk * 5120 + qct * 32);
    } else {
      const int off = (i < 5 ? (WN * 5 + i) * 32 : 18432 + (WN * 5 + i - 5) * 32) + kk * 9216;
      lo = tr_read(sb + woff0 + off);
      hi = tr_read(sb + woff1 + off);
    }
    return __builtin_shufflevector(lo, hi, 0, 1, 2, 3, 4, 5, 6, 7);
  };

  STAMPR(0, 14);
  STAMP(0, 0);
#pragma unroll
  for (int p = 0; p < 10; ++p) stage_piece(0, p);
  STAMP(0, 12);
  int tl = 9;
  asm volatile("" : "+s"(tl));
#pragma clang loop unroll(disable)
  for (int t = 0; t < tl; ++t) {
    asm volatile("s_waitcnt vmcnt(0)" ::: "memory");
    __builtin_amdgcn_s_barrier();
    asm volatile("" ::: "memory");
    STAMP(0, 1 + t);
    const bool issue = t < 8;
    const char* sb = lds + (t & 1) * G1_STAGE;
    bf16x8 xf[2][4], wfa, wfb, wfc;
    wfa = load_w(sb, 0);
#pragma unroll
    for (int rt = 0; rt < 4; ++rt) xf[0][rt] = *(const bf16x8*)(sb + xoff0 + rt * 2048);
    wfb = load_w(sb, 1);
    wfc = load_w(sb, 2);
#pragma unroll
    for (int grp = 0; grp < 22; ++grp) {
      const int kk = grp / 11, i = grp - kk * 11;
      bf16x8 wf = wfa;
      wfa = wfb;
      wfb = wfc;
      if (grp + 3 < 22) wfc = load_w(sb, grp + 3);
      if (grp >= 4 && grp < 8) xf[1][grp - 4] = *(const bf16x8*)(sb + xoff1 + (grp - 4) * 2048);
      if (grp < 10 && issue) stage_piece(t + 1, grp);
      const bool isq = (i == 10) || (WN == 1 && (i == 4 || i == 9));
#pragma unroll
      for (int rt = 0; rt < 4; ++rt) {
        if (ISV && isq) acc[i][rt] = MFMA16(xf[kk][rt], wf, acc[i][rt]);
        else acc[i][rt] = MFMA16(wf, xf[kk][rt], acc[i][rt]);
      }
      __builtin_amdgcn_sched_barrier(0);
    }
  }

  STAMP(0, 10);
  __builtin_amdgcn_sched_barrier(0);
  {
  const int l = lane_id_opaque();
  const int g = l >> 4;
  const int tok0 = bm * 256 + wm * 64;
  constexpr int NP = WN == 0 ? 5 : 4;
#pragma unroll
  for (int i = 0; i < NP; ++i) {
    const int ct = WN * 5 + i;
    const int chunk = j * 18 + ct * 2 + (g >> 1);
#pragma unroll
    for (int rt = 0; rt < 4; ++rt) {
      float hv[4];
#pragma unroll
      for (int r = 0; r < 4; ++r) hv[r] = bf16r(acc[i][rt][r]) * bf16r(acc[i + 5][rt][r]);
      u32x2 o = {pack_bf16(hv[0], hv[1]), pack_bf16(hv[2], hv[3])};
      int token = tok0 + rt * 16 + (l & 15);
      *(u32x2*)(hT + ((size_t)chunk * SEQ + token) * 8 + (g & 1) * 4) = o;
    }
  }
  if (j >= 28) {
    const int tid = w * 64 + l;
    const int token = bm * 256 + (j - 28) * 64 + (tid >> 3), prt = tid & 7;
    const u16* xr = x + (size_t)token * DM + prt * 72;
    const u16* gr = gw + prt * 72;
    bf16x8 xv[9], gv[9];
#pragma unroll
    for (int i = 0; i < 9; ++i) { xv[i] = *(const bf16x8*)(xr + i * 8); gv[i] = *(const bf16x8*)(gr + i * 8); }
    float sum = 0.f;
#pragma unroll
    for (int i = 0; i < 9; ++i)
#pragma unroll
      for (int e = 0; e < 8; ++e) sum += bf16r(bf2f((u16)xv[i][e]) * bf2f((u16)gv[i][e]));
    sum += __shfl_xor(sum, 1);
    sum += __shfl_xor(sum, 2);
    sum += __shfl_xor(sum, 4);
    sum = bf16r(sum);
    if (prt == 0) gout[token] = bf16r(1.0f / (1.0f + __expf(-sum)));
  }
  if (j < 27) {
    if (!ISV) __syncthreads();
    constexpr int NQ = WN == 0 ? 1 : 3;
#pragma unroll
    for (int qi = 0; qi < NQ; ++qi) {
      const int ai = WN == 0 ? 10 : (qi == 0 ? 4 : (qi == 1 ? 9 : 10));
      const int qct = WN == 0 ? 0 : qi + 1;
      if (ISV) {
        int cv = j * 64 + qct * 16 + (l & 15) - 1152;
        int head = cv / 18, d = cv - head * 18;
#pragma unroll
        for (int rt = 0; rt < 4; ++rt) {
          int t16 = tok0 + rt * 16;
          int tile32 = t16 >> 5, s = rt & 1;
          char* dst = KV + (size_t)(head * 64 + tile32) * KVREC + 1024 + ((s * 2 + (g & 1)) * 19 + d) * 16 + (g >> 1) * 8;
          u32x2 o = {pack_bf16(acc[ai][rt][0], acc[ai][rt][1]), pack_bf16(acc[ai][rt][2], acc[ai][rt][3])};
          *(u32x2*)dst = o;
          if (d == 17) *(u32x2*)(dst + 16) = (u32x2){0x3F803F80u, 0x3F803F80u};
        }
      } else {
        const float sc = j < 9 ? 0.23570226039551584f * 1.4426950408889634f : 1.0f;
#pragma unroll
        for (int rt = 0; rt < 4; ++rt) {
          u32x2 o = {pack_bf16(acc[ai][rt][0] * sc, acc[ai][rt][1] * sc), pack_bf16(acc[ai][rt][2] * sc, acc[ai][rt][3] * sc)};
          *(u32x2*)(lds + (wm * 64 + rt * 16 + (l & 15)) * 136 + qct * 32 + g * 8) = o;
        }
      }
    }
    if (!ISV) {
      __syncthreads();
      const bool isQ = j < 9;
      const int tid = w * 64 + l;
      const int pr = tid & 31;
      const int c = j * 64 + 2 * pr - (isQ ? 0 : 576);
      const int head = c / 18, d = c - head * 18;
#pragma unroll 4
      for (int it = 0; it < 16; ++it) {
        const int tl_ = it * 16 + (tid >> 5);
        const unsigned v = *(const unsigned*)(lds + tl_ * 136 + pr * 4);
        const int token = bm * 256 + tl_;
        char* dst;
        if (isQ) dst = (char*)(Qb + ((size_t)head * SEQ + token) * 32 + d);
        else {
          char* rec = KV + (size_t)(head * 64 + (token >> 5)) * KVREC;
          dst = d < 16 ? rec + (token & 31) * 32 + d * 2 : rec + 2240 + (token & 31) * 4;
        }
        *(unsigned*)dst = v;
      }
    }
  }
  }
  STAMP(0, 11);
  STAMPR(0, 15);
}

__global__ __launch_bounds__(512, 2) void gemm1_kernel(const u16* __restrict__ x, const u16* __restrict__ wg,
                                                       const u16* __restrict__ wu, const u16* __restrict__ wqkv,
                                                       u16* __restrict__ hT, u16* __restrict__ Qb,
                                                       char* __restrict__ KV, const u16* __restrict__ gw,
                                                       float* __restrict__ gout) {
  __shared__ __attribute__((aligned(16))) char lds[2 * G1_STAGE + 256];
  const int b = blockIdx.x;
  const int xcd = b & 7, idx = b >> 3;
  const int j = xcd * 4 + (idx & 3), bm = idx >> 2;
  const int w = __builtin_amdgcn_readfirstlane(threadIdx.x >> 6);
  const bool isv = (j >= 18);
  if (w & 1) {
    if (isv) gemm1_body<true, 1>(x, wg, wu, wqkv, hT, Qb, KV, gw, gout, lds, bm, j, w);
    else gemm1_body<false, 1>(x, wg, wu, wqkv, hT, Qb, KV, gw, gout, lds, bm, j, w);
  } else {
    if (isv) gemm1_body<true, 0>(x, wg, wu, wqkv, hT, Qb, KV, gw, gout, lds, bm, j, w);
    else gemm1_body<false, 0>(x, wg, wu, wqkv, hT, Qb, KV, gw, gout, lds, bm, j, w);
  }
}

DI float xhalf_max(float v) {
  auto rr = __builtin_amdgcn_permlane32_swap(__float_as_uint(v), __float_as_uint(v), false, false);
  return fmaxf(__uint_as_float(rr[0]), __uint_as_float(rr[1]));
}
DI float tile_max(const f32x16& S) {
  float t = fmaxf(fmaxf(S[0], S[1]), S[2]);
#pragma unroll
  for (int i = 3; i < 15; i += 2) t = fmaxf(fmaxf(t, S[i]), S[i + 1]);
  return xhalf_max(fmaxf(t, S[15]));
}
DI f32x16 qk_tile(bf16x8 kA, unsigned kH, bf16x8 qA, u32x4 qB) {
  f32x16 S;
#pragma unroll
  for (int i = 0; i < 16; ++i) S[i] = 0.f;
  u32x4 kBv = {kH, 0x00003F80u, 0u, 0u};
  S = MFMA32(kA, qA, S);
  S = MFMA32(__builtin_bit_cast(bf16x8, kBv), __builtin_bit_cast(bf16x8, qB), S);
  return S;
}
DI void pv_tile(f32x16& S, bf16x8 v0, bf16x8 v1, f32x16& O) {
#pragma unroll
  for (int i = 0; i < 16; ++i) S[i] = __builtin_amdgcn_exp2f(S[i]);
  u32x4 p0 = {pack_bf16(S[0], S[1]), pack_bf16(S[2], S[3]), pack_bf16(S[4], S[5]), pack_bf16(S[6], S[7])};
  u32x4 p1 = {pack_bf16(S[8], S[9]), pack_bf16(S[10], S[11]), pack_bf16(S[12], S[13]), pack_bf16(S[14], S[15])};
  O = MFMA32(v0, __builtin_bit_cast(bf16x8, p0), O);
  O = MFMA32(v1, __builtin_bit_cast(bf16x8, p1), O);
}

constexpr int AT_STAGE = 2 * 8 * KVREC;

DI void attn_body(const u16* __restrict__ Qb, const char* __restrict__ KV, u16* __restrict__ oT, char* lds,
                  const int b) {
  const int head = (b & 7) * 4 + ((b >> 3) & 3), qblk = b >> 5;
  const int w = __builtin_amdgcn_readfirstlane(threadIdx.x >> 6);
  const int l = threadIdx.x & 63, r = l & 31, h = l >> 5;
  const int qp = w & 3, kvh = w >> 2;
  const int token0 = qblk * 256 + qp * 64 + r;

  const unsigned lds_base = (unsigned)(size_t)(LDS_AS char*)lds;
  const char* kvsrc = KV + (size_t)(head * 64) * KVREC;
  auto stage = [&](int s) {
#pragma unroll
    for (int i = 0; i < 5; ++i) {
      int n = w * 5 + i;
      if (n > 37) n -= 2;
      const int hf = n >= 19 ? 1 : 0, pc = n - hf * 19;
      glds16_asm((unsigned)(l * 16), kvsrc + (hf * 32 + s * 8) * KVREC + pc * 1024,
                 lds_base + (s & 1) * AT_STAGE + hf * (8 * KVREC) + pc * 1024);
    }
  };
  STAMPR(1, 14);
  STAMP(1, 0);
  stage(0);

  bf16x8 qA[2];
  u32x4 qB[2];
  float m[2];
  f32x16 O[2];
#pragma unroll
  for (int q = 0; q < 2; ++q) {
    const u16* qrow = Qb + ((size_t)head * SEQ + token0 + q * 32) * 32;
    qA[q] = *(const bf16x8*)(qrow + 8 * h);
    unsigned q67 = *(const unsigned*)(qrow + 16);
    asm volatile("" : "+v"(qA[q]), "+v"(q67));
    qB[q] = (u32x4){h ? 0u : q67, 0u, 0u, 0u};
    m[q] = 0.f;
#pragma unroll
    for (int i = 0; i < 16; ++i) O[q][i] = 0.f;
  }
  bool failed = false;

  const int koff = kvh * (8 * KVREC) + r * 32 + h * 16;
  const int hoff = kvh * (8 * KVREC) + 2240 + r * 4;
  const int voff = kvh * (8 * KVREC) + 1024 + (h * 19 + (r < 18 ? r : 18)) * 16;

#pragma clang loop unroll(disable)
  for (int s = 0; s < 4; ++s) {
    asm volatile("s_waitcnt vmcnt(0)" ::: "memory");
    __builtin_amdgcn_s_barrier();
    asm volatile("" ::: "memory");
    STAMP(1, 1 + s);
    if (s < 3) stage(s + 1);
    const char* sb = lds + (s & 1) * AT_STAGE;
    bf16x8 kA = *(const bf16x8*)(sb + koff);
    unsigned kH = *(const unsigned*)(sb + hoff);
    if (s == 0) {
#pragma unroll
      for (int q = 0; q < 2; ++q) {
        f32x16 S0 = qk_tile(kA, kH, qA[q], qB[q]);
        m[q] = bf16r(tile_max(S0));
        qB[q][1] = h ? 0u : pack_bf16(-m[q], 0.f);
      }
    }
    f32x16 Sa = qk_tile(kA, kH, qA[0], qB[0]);
#pragma unroll
    for (int i = 0; i < 8; ++i) {
      bf16x8 v0 = *(const bf16x8*)(sb + voff + i * KVREC);
      bf16x8 v1 = *(const bf16x8*)(sb + voff + i * KVREC + 608);
      f32x16 Sb = qk_tile(kA, kH, qA[1], qB[1]);
      if (i < 7) {
        kA = *(const bf16x8*)(sb + koff + (i + 1) * KVREC);
        kH = *(const unsigned*)(sb + hoff + (i + 1) * KVREC);
      }
      pv_tile(Sa, v0, v1, O[0]);
      if (i < 7) Sa = qk_tile(kA, kH, qA[0], qB[0]);
      pv_tile(Sb, v0, v1, O[1]);
    }
    float lmax = h ? 0.f : fmaxf(O[0][10], O[1][10]);
    if (__any(!(lmax < 4294967296.f) || !(O[0][10] == O[0][10]) || !(O[1][10] == O[1][10]))) {
#pragma unroll
      for (int q = 0; q < 2; ++q) {
        float lv = O[q][10];
        auto rr = __builtin_amdgcn_permlane32_swap(__float_as_uint(lv), __float_as_uint(lv), false, false);
        lv = __uint_as_float(rr[0]);
        if (__any(!(lv < 3.0e38f))) failed = true;
        float sh = lv > 1.f && lv < 3.0e38f ? floorf(__builtin_amdgcn_logf(lv)) : 0.f;
        float mn = bf16r(m[q] + sh);
        float sc = __builtin_amdgcn_exp2f(m[q] - mn);
        m[q] = mn;
#pragma unroll
        for (int i = 0; i < 16; ++i) O[q][i] *= sc;
        qB[q][1] = h ? 0u : pack_bf16(-mn, 0.f);
      }
    }
  }

  STAMP(1, 5);
  if (failed) {
#pragma clang loop unroll(disable)
    for (int q = 0; q < 2; ++q) {
      const char* rec = KV + (size_t)(head * 64 + kvh * 32) * KVREC;
      const bf16x8 qa = q ? qA[1] : qA[0];
      u32x4 qb = q ? qB[1] : qB[0];
      qb[1] = 0u;
      float mm = -3.0e38f;
      f32x16 Oo;
#pragma unroll
      for (int i = 0; i < 16; ++i) Oo[i] = 0.f;
#pragma clang loop unroll(disable)
      for (int it = 0; it < 32; ++it, rec += KVREC) {
        bf16x8 kA = *(const bf16x8*)(rec + r * 32 + h * 16);
        unsigned kH = *(const unsigned*)(rec + 2240 + r * 4);
        bf16x8 v0 = *(const bf16x8*)(rec + 1024 + (h * 19 + (r < 18 ? r : 18)) * 16);
        bf16x8 v1 = *(const bf16x8*)(rec + 1024 + 608 + (h * 19 + (r < 18 ? r : 18)) * 16);
        f32x16 S = qk_tile(kA, kH, qa, qb);
        float mn = fmaxf(mm, tile_max(S));
        float sc = __builtin_amdgcn_exp2f(mm - mn);
        mm = mn;
#pragma unroll
        for (int i = 0; i < 16; ++i) { S[i] -= mn; Oo[i] *= sc; }
        pv_tile(S, v0, v1, Oo);
      }
      if (q) { O[1] = Oo; m[1] = mm; } else { O[0] = Oo; m[0] = mm; }
    }
  }

  __syncthreads();
  float* mrg = (float*)lds;
  if (kvh == 1) {
#pragma unroll
    for (int q = 0; q < 2; ++q) {
#pragma unroll
      for (int i = 0; i < 12; ++i) mrg[((qp * 2 + q) * 13 + i) * 64 + l] = O[q][i];
      mrg[((qp * 2 + q) * 13 + 12) * 64 + l] = m[q];
    }
  }
  __syncthreads();
  if (kvh == 0) {
#pragma unroll
    for (int q = 0; q < 2; ++q) {
      float m1 = mrg[((qp * 2 + q) * 13 + 12) * 64 + l];
      float mm = fmaxf(m[q], m1);
      float a0 = __builtin_amdgcn_exp2f(m[q] - mm), a1 = __builtin_amdgcn_exp2f(m1 - mm);
      float o[12];
#pragma unroll
      for (int i = 0; i < 12; ++i) o[i] = O[q][i] * a0 + mrg[((qp * 2 + q) * 13 + i) * 64 + l] * a1;
      float lsum = __shfl(o[10], r);
      float inv = 1.0f / lsum;
      const int token = token0 + q * 32;
#pragma unroll
      for (int pr = 0; pr < 5; ++pr) {
        if (pr == 4 && h) continue;
        int d = (pr >> 1) * 8 + (pr & 1) * 2 + 4 * h;
        int c = head * 18 + d;
        unsigned v = pack_bf16(o[2 * pr] * inv, o[2 * pr + 1] * inv);
        *(unsigned*)(oT + ((size_t)(c >> 3) * SEQ + token) * 8 + (c & 7)) = v;
      }
    }
  }
  STAMP(1, 6);
  STAMPR(1, 15);
}

template <int N> DI void wait_vm() { asm volatile("s_waitcnt vmcnt(%0)" ::"i"(N) : "memory"); }
DI void raw_barrier() {
  __builtin_amdgcn_s_barrier();
  asm volatile("" ::: "memory");
}

template <int NW, int RT, int NS = 4>
struct Gemm2 {
  static constexpr int BM = NW * RT * 16;
  static constexpr int AB = 8 * BM * 16;
  static constexpr int STG = AB + 18432;
  static constexpr int NAW = AB / 1024 / NW;
  static constexpr int NWW = (18 + NW - 1) / NW;
  static constexpr int NV = NAW + NWW;
  static constexpr int LA = 4;

  const char* abase;
  const char* wbase;
  unsigned avo[NAW], wvo[NWW];
  unsigned lds_base;
  char* lds;
  int w, aoff, woff0, woff1;
  bf16x8 af[2][RT], wq_[LA];

  DI void init(const u16* AT, const u16* W, int tok0, int col0, int kbeg, char* lds_, int w_, int l) {
    lds = lds_; w = w_;
    lds_base = (unsigned)(size_t)(LDS_AS char*)lds_;
    abase = (const char*)(AT + (size_t)(kbeg / 8) * SEQ * 8);
    wbase = (const char*)(W + (size_t)kbeg * DM);
    const int g = l >> 4;
#pragma unroll
    for (int i = 0; i < NAW; ++i) {
      int off = (w * NAW + i) * 64 + l;
      int chunk = off / BM, token = off - chunk * BM;
      avo[i] = (unsigned)((chunk * SEQ + tok0 + token) * 16);
    }
#pragma unroll
    for (int i = 0; i < NWW; ++i) {
      int n = w + NW * i;
      if (n >= 18) n = 16 + (n & 1);
      int c = n * 64 + l;
      int lr = c / 18, ch = c - lr * 18;
      int krow = lr ^ ((lr & 8) >> 1);
      wvo[i] = (unsigned)((krow * DM + col0 + ch * 8) * 2);
    }
    aoff = g * (BM * 16) + (w * RT * 16 + (l & 15)) * 16;
    const int wq = (l & 15) >> 2, wp = l & 3;
    woff0 = AB + (g * 8 + 4 * (0 ^ (g & 1)) + wq) * 288 + wp * 8;
    woff1 = AB + (g * 8 + 4 * (1 ^ (g & 1)) + wq) * 288 + wp * 8;
  }
  DI void piece(int t, int p) {
    const unsigned sb = lds_base + (t & (NS - 1)) * STG;
    if (p < NAW) {
      glds16_asm(avo[p], abase + (size_t)t * (8 * SEQ * 16), sb + (w * NAW + p) * 1024);
    } else {
      const int i = p - NAW;
      int n = w + NW * i;
      if (n >= 18) n = 16 + (n & 1);
      glds16_asm(wvo[i], wbase + (size_t)t * (64 * DM * 2), sb + AB + n * 1024);
    }
  }
  DI bf16x8 load_w(const char* sb, int grp) {
    const int kk = grp / 9, ct = grp - kk * 9;
    s16x4 lo = tr_read(sb + woff0 + kk * 9216 + ct * 32);
    s16x4 hi = tr_read(sb + woff1 + kk * 9216 + ct * 32);
    return __builtin_shufflevector(lo, hi, 0, 1, 2, 3, 4, 5, 6, 7);
  }
  DI bf16x8 load_a(const char* sb, int kk, int rt) {
    return *(const bf16x8*)(sb + aoff + kk * 4 * BM * 16 + rt * 256);
  }
  DI void prologue_issue() {
#pragma unroll
    for (int t = 0; t < 3; ++t)
#pragma unroll
      for (int p = 0; p < NV; ++p) piece(t, p);
  }
  DI void prologue_read() {
    wait_vm<2 * NV>();
    raw_barrier();
#pragma unroll
    for (int kk = 0; kk < 2; ++kk)
#pragma unroll
      for (int rt = 0; rt < RT; ++rt) af[kk][rt] = load_a(lds, kk, rt);
#pragma unroll
    for (int i = 0; i < LA; ++i) wq_[i] = load_w(lds, i);
  }
  template <bool ISSUE, bool PREF>
  DI void body(int t, f32x4 (&acc)[9][RT]) {
    const char* sb = lds + (t & 3) * STG;
    const char* sn = lds + ((t + 1) & 3) * STG;
    bf16x8 an[2][RT];
#pragma unroll
    for (int grp = 0; grp < 18; ++grp) {
      bf16x8 wf = wq_[0];
#pragma unroll
      for (int i = 0; i + 1 < LA; ++i) wq_[i] = wq_[i + 1];
      if (grp + LA < 18) wq_[LA - 1] = load_w(sb, grp + LA);
      else if (PREF) wq_[LA - 1] = load_w(sn, grp + LA - 18);
      if (PREF && grp >= 8 && grp < 8 + 2 * RT) an[(grp - 8) / RT][(grp - 8) % RT] = load_a(sn, (grp - 8) / RT, (grp - 8) % RT);
      if (ISSUE && grp < NV) piece(t + 3, grp);
      const int kk = grp / 9, ct = grp - kk * 9;
#pragma unroll
      for (int rt = 0; rt < RT; ++rt) acc[ct][rt] = MFMA16(wf, af[kk][rt], acc[ct][rt]);
      __builtin_amdgcn_sched_barrier(0);
    }
    if (PREF) {
#pragma unroll
      for (int kk = 0; kk < 2; ++kk)
#pragma unroll
        for (int rt = 0; rt < RT; ++rt) af[kk][rt] = an[kk][rt];
    }
  }
  template <bool ISSUE>
  DI void body2(int t, f32x4 (&acc)[9][RT]) {
    const char* sb = lds + (t & 1) * STG;
#pragma unroll
    for (int kk = 0; kk < 2; ++kk)
#pragma unroll
      for (int rt = 0; rt < RT; ++rt) af[kk][rt] = load_a(sb, kk, rt);
#pragma unroll
    for (int i = 0; i < LA; ++i) wq_[i] = load_w(sb, i);
#pragma unroll
    for (int grp = 0; grp < 18; ++grp) {
      bf16x8 wf = wq_[0];
#pragma unroll
      for (int i = 0; i + 1 < LA; ++i) wq_[i] = wq_[i + 1];
      if (grp + LA < 18) wq_[LA - 1] = load_w(sb, grp + LA);
      if (ISSUE && grp < NV) piece(t + 1, grp);
      const int kk = grp / 9, ct = grp - kk * 9;
#pragma unroll
      for (int rt = 0; rt < RT; ++rt) acc[ct][rt] = MFMA16(wf, af[kk][rt], acc[ct][rt]);
      __builtin_amdgcn_sched_barrier(0);
    }
  }
  DI void run2(int nsteps, f32x4 (&acc)[9][RT]) {
    int tl = nsteps - 1;
    asm volatile("" : "+s"(tl));
#pragma clang loop unroll(disable)
    for (int t = 0; t < tl; ++t) {
      wait_vm<0>();
      raw_barrier();
      body2<true>(t, acc);
    }
    wait_vm<0>();
    raw_barrier();
    body2<false>(tl, acc);
  }
  DI void run(int nsteps, f32x4 (&acc)[9][RT]) {
    prologue_read();
    int tl = nsteps - 3;
    asm volatile("" : "+s"(tl));
#pragma clang loop unroll(disable)
    for (int t = 0; t < tl; ++t) {
      wait_vm<NV>();
      raw_barrier();
      body<true, true>(t, acc);
    }
    wait_vm<NV>();
    raw_barrier();
    body<false, true>(tl, acc);
    wait_vm<0>();
    raw_barrier();
    body<false, true>(tl + 1, acc);
    body<false, false>(tl + 2, acc);
  }
};

DI void down_body(const u16* __restrict__ hT, const u16* __restrict__ wd, u16* __restrict__ part, char* lds,
                  const int b) {
  using G = Gemm2<8, 1, 2>;
  const int xcd = b & 7, idx = b >> 3;
  const int ks = xcd >> 1;
  const int bm = (xcd & 1) * 8 + (idx >> 2);
  const int bn = idx & 3;
  const int w = __builtin_amdgcn_readfirstlane(threadIdx.x >> 6);
  const int l = threadIdx.x & 63;
  STAMPR(2, 14);
  STAMP(2, 0);
  G gm;
  gm.init(hT, wd, bm * 128, bn * 144, ks * 1152, lds, w, l);
#pragma unroll
  for (int p = 0; p < G::NV; ++p) gm.piece(0, p);
  STAMP(2, 1);
  f32x4 acc[9][1];
#pragma unroll
  for (int i = 0; i < 9; ++i) acc[i][0] = (f32x4){0.f, 0.f, 0.f, 0.f};
  gm.run2(18, acc);
  STAMP(2, 2);
#pragma unroll
  for (int ct = 0; ct < 9; ++ct) {
    int tt = bm * 8 + w, ctg = bn * 9 + ct;
    u32x2 pv = {pack_bf16(acc[ct][0][0], acc[ct][0][1]), pack_bf16(acc[ct][0][2], acc[ct][0][3])};
    *(u32x2*)(part + ((size_t)(ks * 128 + tt) * 36 + ctg) * 256 + l * 4) = pv;
  }
  STAMP(2, 3);
  STAMPR(2, 15);
}

__global__ __launch_bounds__(512, 4) void mid_kernel(const u16* __restrict__ Qb, const char* __restrict__ KV,
                                                     u16* __restrict__ oT, const u16* __restrict__ hT,
                                                     const u16* __restrict__ wd, u16* __restrict__ part) {
  __shared__ __attribute__((aligned(16))) char lds[2 * AT_STAGE];
  const int b = blockIdx.x;
  if (b >= 256) down_body(hT, wd, part, lds, b - 256);
  else attn_body(Qb, KV, oT, lds, b);
}

__global__ __launch_bounds__(256, 2) void final_kernel(const u16* __restrict__ oT, const u16* __restrict__ wo,
                                                       const u16* __restrict__ part, const float* __restrict__ gin,
                                                       u16* __restrict__ out) {
  __shared__ __attribute__((aligned(16))) char lds[576 * 96 + 2048];
  const int b = blockIdx.x;
  const int idx = b >> 3;
  const int bm = (b & 7) * 4 + idx / 12, bn = idx % 12;
  const int w = __builtin_amdgcn_readfirstlane(threadIdx.x >> 6);
  const int l = threadIdx.x & 63, g = l >> 4;
  STAMPR(3, 14);
  STAMP(3, 0);
  const unsigned lds_base = (unsigned)(size_t)(LDS_AS char*)lds;
#pragma unroll
  for (int i = 0; i < 14; ++i) {
    const int n = w * 14 + i;
    int c = n * 64 + l;
    int lr = c / 6, ch = c - lr * 6;
    if (lr > 575) lr = 575;
    int krow = lr ^ ((lr & 8) >> 1);
    glds16_asm((unsigned)((krow * DM + bn * 48 + ch * 8) * 2), wo, lds_base + n * 1024);
  }
  const int token = bm * 64 + w * 16 + (l & 15);
  bf16x8 af[18];
#pragma unroll
  for (int ks = 0; ks < 18; ++ks) af[ks] = *(const bf16x8*)(oT + ((size_t)(ks * 4 + g) * SEQ + token) * 8);
  float gg = gin[token];
  const int tt = bm * 4 + w;
  f32x4 bsum[3];
#pragma unroll
  for (int ct = 0; ct < 3; ++ct) {
    int ctg = bn * 3 + ct;
    f32x4 pp[4];
#pragma unroll
    for (int ks = 0; ks < 4; ++ks) {
      u32x2 v = *(const u32x2*)(part + ((size_t)(ks * 128 + tt) * 36 + ctg) * 256 + l * 4);
      pp[ks] = (f32x4){__uint_as_float(v[0] << 16), __uint_as_float(v[0] & 0xffff0000u),
                       __uint_as_float(v[1] << 16), __uint_as_float(v[1] & 0xffff0000u)};
    }
    bsum[ct] = (pp[0] + pp[1]) + (pp[2] + pp[3]);
  }
  STAMP(3, 1);
  asm volatile("s_waitcnt vmcnt(0)" ::: "memory");
  __builtin_amdgcn_s_barrier();
  asm volatile("" ::: "memory");
  STAMP(3, 2);
  const int wq = (l & 15) >> 2, wp = l & 3;
  const int woff0 = (g * 8 + 4 * (0 ^ (g & 1)) + wq) * 96 + wp * 8;
  const int woff1 = (g * 8 + 4 * (1 ^ (g & 1)) + wq) * 96 + wp * 8;
  f32x4 acc[3];
#pragma unroll
  for (int i = 0; i < 3; ++i) acc[i] = (f32x4){0.f, 0.f, 0.f, 0.f};
#pragma unroll
  for (int ks = 0; ks < 18; ++ks)
#pragma unroll
    for (int ct = 0; ct < 3; ++ct) {
      s16x4 lo = tr_read(lds + woff0 + ks * 3072 + ct * 32);
      s16x4 hi = tr_read(lds + woff1 + ks * 3072 + ct * 32);
      bf16x8 wf = __builtin_shufflevector(lo, hi, 0, 1, 2, 3, 4, 5, 6, 7);
      acc[ct] = MFMA16(wf, af[ks], acc[ct]);
    }
  STAMP(3, 3);
  const float og = bf16r(1.0f - gg);
#pragma unroll
  for (int ct = 0; ct < 3; ++ct) {
    int ctg = bn * 3 + ct;
    float o[4];
#pragma unroll
    for (int e = 0; e < 4; ++e) o[e] = bf16r(gg * bf16r(acc[ct][e])) + bf16r(og * bf16r(bsum[ct][e]));
    u32x2 ov = {pack_bf16(o[0], o[1]), pack_bf16(o[2], o[3])};
    *(u32x2*)(out + (size_t)token * DM + ctg * 16 + g * 4) = ov;
  }
  STAMP(3, 4);
  STAMPR(3, 15);
}

extern "C" void kernel_launch(void* const* d_in, const int* in_sizes, int n_in, void* d_out, int out_size,
                              void* d_ws, size_t ws_size, hipStream_t stream) {
  const u16* x = (const u16*)d_in[0];
  const u16* wqkv = (const u16*)d_in[1];
  const u16* wo = (const u16*)d_in[2];
  const u16* wg = (const u16*)d_in[4];
  const u16* wu = (const u16*)d_in[5];
  const u16* wd = (const u16*)d_in[6];
  const u16* gw = (const u16*)d_in[7];
  char* ws = (char*)d_ws;
  u16* hT = (u16*)(ws + WS_HT);
  u16* Qb = (u16*)(ws + WS_QB);
  char* KV = ws + WS_KV;
  u16* oT = (u16*)(ws + WS_OT);
  u16* part = (u16*)(ws + WS_PART);
  float* gbuf = (float*)(ws + WS_G);
  u16* out = (u16*)d_out;

  gemm1_kernel<<<256, 512, 0, stream>>>(x, wg, wu, wqkv, hT, Qb, KV, gw, gbuf);
  mid_kernel<<<512, 512, 0, stream>>>(Qb, KV, oT, hT, wd, part);
  final_kernel<<<384, 256, 0, stream>>>(oT, wo, part, gbuf, out);
}
```

```cpp
#include <hip/hip_runtime.h>
#include <hip/hip_bf16.h>
#include <stdint.h>
#include <type_traits>

typedef unsigned short u16;
typedef __attribute__((ext_vector_type(8))) short bf16x8;
typedef __attribute__((ext_vector_type(4))) short s16x4;
typedef __attribute__((ext_vector_type(4))) float f32x4;
typedef __attribute__((ext_vector_type(16))) float f32x16;
typedef __attribute__((ext_vector_type(2))) float f32x2;
typedef __attribute__((ext_vector_type(2))) __bf16 bf16x2_t;
typedef __attribute__((ext_vector_type(2))) unsigned u32x2;
typedef __attribute__((ext_vector_type(4))) unsigned u32x4;

#define DI __device__ __forceinline__
#define LDS_AS __attribute__((address_space(3)))

constexpr int SEQ = 2048, DM = 576, FF = 4608, NH = 32, DH = 18, NQKV = 1728;

DI unsigned pack_bf16(float lo, float hi) {
  f32x2 f = {lo, hi};
  bf16x2_t b = __builtin_convertvector(f, bf16x2_t);
  return __builtin_bit_cast(unsigned, b);
}
DI float bf16r(float x) { return __uint_as_float(pack_bf16(x, x) << 16); }
DI float bf2f(u16 v) { return __uint_as_float(((unsigned)v) << 16); }

DI s16x4 tr_read(const char* lds_ptr) {
  return __builtin_amdgcn_ds_read_tr16_b64_v4i16((LDS_AS s16x4*)(lds_ptr));
}
DI void glds16(const void* gsrc, char* lds_dst) {
  __builtin_amdgcn_global_load_lds((const __attribute__((address_space(1))) void*)gsrc,
                                   (LDS_AS void*)lds_dst, 16, 0, 0);
}
DI int lane_id_opaque() {
  int l;
  asm volatile("v_mbcnt_lo_u32_b32 %0, -1, 0\n\tv_mbcnt_hi_u32_b32 %0, -1, %0" : "=v"(l));
  return l;
}
DI void glds16_asm(unsigned voff, const void* sbase, unsigned lds_dst) {
  unsigned keep;
  asm volatile("s_mov_b32 %0, m0\n\ts_mov_b32 m0, %3\n\ts_nop 0\n\tglobal_load_lds_dwordx4 %1, %2\n\ts_mov_b32 m0, %0"
               : "=&s"(keep) : "v"(voff), "s"(sbase), "s"(lds_dst) : "memory");
}
DI void touch_line(const void* gptr, unsigned lds_scratch) {
  unsigned keep;
  asm volatile("s_mov_b32 %0, m0\n\ts_mov_b32 m0, %2\n\ts_nop 0\n\tglobal_load_lds_dword %1, off\n\ts_mov_b32 m0, %0"
               : "=&s"(keep) : "v"(gptr), "s"(lds_scratch) : "memory");
}
DI void touch_line_s(unsigned voff, const void* sbase, unsigned lds_scratch) {
  unsigned keep;
  asm volatile("s_mov_b32 %0, m0\n\ts_mov_b32 m0, %3\n\ts_nop 0\n\tglobal_load_lds_dword %1, %2\n\ts_mov_b32 m0, %0"
               : "=&s"(keep) : "v"(voff), "s"(sbase), "s"(lds_scratch) : "memory");
}
#define STAMP(kid, i) do {} while (0)
#define STAMPR(kid, i) do {} while (0)
#define MFMA16(a, b, c) __builtin_amdgcn_mfma_f32_16x16x32_bf16((a), (b), (c), 0, 0, 0)
#define MFMA32(a, b, c) __builtin_amdgcn_mfma_f32_32x32x16_bf16((a), (b), (c), 0, 0, 0)

constexpr size_t WS_HT = 0;
constexpr size_t WS_QB = WS_HT + (size_t)FF * SEQ * 2;
constexpr int KVREC = 2432;
constexpr size_t WS_KV = WS_QB + (size_t)NH * SEQ * 32 * 2;
constexpr size_t WS_VF = WS_KV;
constexpr size_t WS_OT = WS_KV + (size_t)NH * 64 * KVREC;
constexpr size_t WS_PART = WS_OT + (size_t)DM * SEQ * 2;
constexpr size_t WS_G = WS_PART + (size_t)4 * SEQ * DM * 4;
constexpr size_t WS_END = WS_G + SEQ * 4;

constexpr int G1_XB = 256 * 128;
constexpr int G1_WB = 18432 * 2 + 10240;
constexpr int G1_STAGE = G1_XB + G1_WB;

template <bool ISV, int WN>
DI void gemm1_body(const u16* __restrict__ x, const u16* __restrict__ wg, const u16* __restrict__ wu,
                   const u16* __restrict__ wqkv, u16* __restrict__ hT, u16* __restrict__ Qb,
                   char* __restrict__ KV, const u16* __restrict__ gw, float* __restrict__ gout, const u16* wd, const u16* wo, char* lds,
                   int bm, int j, int w) {
  const int l = lane_id_opaque();
  const int wm = w >> 1;
  const int g = l >> 4;
  const int jq = j < 27 ? j : 26;

  unsigned xvo, guvo[2], evo[2];
  const char* ebase[2];
  int estep, edst[2];
  {
    int row = 32 * w + (l >> 3);
    int chunk = (l & 7) ^ (l >> 3);
    xvo = (unsigned)((bm * 256 + row) * DM + chunk * 8) * 2u;
#pragma unroll
    for (int i = 0; i < 2; ++i) {
      int c = (w + 8 * i) * 64 + l;
      int lr = c / 18, ch = c - lr * 18;
      int krow = lr ^ ((lr & 8) >> 1);
      guvo[i] = (unsigned)(krow * FF + j * 144 + ch * 8) * 2u;
    }
    if (w < 2) {
      int c = (16 + w) * 64 + l;
      int lr = c / 18, ch = c - lr * 18;
      int krow = lr ^ ((lr & 8) >> 1);
      evo[0] = evo[1] = (unsigned)(krow * FF + j * 144 + ch * 8) * 2u;
      ebase[0] = (const char*)wg; ebase[1] = (const char*)wu;
      estep = 64 * FF * 2;
      edst[0] = G1_XB + (16 + w) * 1024; edst[1] = G1_XB + 18432 + (16 + w) * 1024;
    } else {
#pragma unroll
      for (int i = 0; i < 2; ++i) {
        int n = (w - 2) + 6 * i;
        if (n > 9) n = 9;
        int c = n * 64 + l;
        int lr = c / 10, ch = c - lr * 10;
        if (ch > 7) ch = 7;
        int krow = lr ^ ((lr & 8) >> 1);
        evo[i] = (unsigned)(krow * NQKV + jq * 64 + ch * 8) * 2u;
        ebase[i] = (const char*)wqkv;
        edst[i] = G1_XB + 36864 + n * 1024;
      }
      estep = 64 * NQKV * 2;
    }
    edst[0] = __builtin_amdgcn_readfirstlane(edst[0]);
    edst[1] = __builtin_amdgcn_readfirstlane(edst[1]);
  }

  const unsigned lds_base = (unsigned)(size_t)(LDS_AS char*)lds;
  auto stage_piece = [&](int t, int p) {
    const unsigned sb = lds_base + (t & 1) * G1_STAGE;
    if (p < 4) {
      const char* xb = (const char*)x + t * 128 + p * 8 * DM * 2;
      glds16_asm(xvo, xb, sb + (32 * w + 8 * p) * 128);
    } else if (p < 8) {
      const int i = (p - 4) >> 1;
      const char* gb = (const char*)((p & 1) ? wu : wg) + (size_t)t * (64 * FF * 2);
      glds16_asm(guvo[i], gb, sb + G1_XB + (p & 1) * 18432 + (w + 8 * i) * 1024);
    } else {
      glds16_asm(evo[p & 1], ebase[p & 1] + (size_t)t * estep, sb + edst[p & 1]);
    }
  };

  f32x4 acc[11][4];
#pragma unroll
  for (int i = 0; i < 11; ++i)
#pragma unroll
    for (int r = 0; r < 4; ++r) acc[i][r] = (f32x4){0.f, 0.f, 0.f, 0.f};

  const int xoff0 = wm * 8192 + (l & 15) * 128 + (((0 + g) ^ (l & 7)) << 4);
  const int xoff1 = wm * 8192 + (l & 15) * 128 + (((4 + g) ^ (l & 7)) << 4);
  const int wq = (l & 15) >> 2, wp = l & 3;
  const int lr0 = g * 8 + 4 * (0 ^ (g & 1)) + wq, lr1 = g * 8 + 4 * (1 ^ (g & 1)) + wq;
  const int woff0 = G1_XB + lr0 * 288 + wp * 8, woff1 = G1_XB + lr1 * 288 + wp * 8;
  const int qoff0 = G1_XB + 36864 + lr0 * 160 + wp * 8, qoff1 = G1_XB + 36864 + lr1 * 160 + wp * 8;

  auto load_w = [&](const char* sb, int grp) -> bf16x8 {
    const int kk = grp / 11, i = grp - kk * 11;
    const bool isq = (i == 10) || (WN == 1 && (i == 4 || i == 9));
    s16x4 lo, hi;
    if (isq) {
      const int qct = WN == 0 ? 0 : (i == 4 ? 1 : (i == 9 ? 2 : 3));
      lo = tr_read(sb + qoff0 + kk * 5120 + qct * 32);
      hi = tr_read(sb + qoff1 + kk * 5120 + qct * 32);
    } else {
      const int off = (i < 5 ? (WN * 5 + i) * 32 : 18432 + (WN * 5 + i - 5) * 32) + kk * 9216;
      lo = tr_read(sb + woff0 + off);
      hi = tr_read(sb + woff1 + off);
    }
    return __builtin_shufflevector(lo, hi, 0, 1, 2, 3, 4, 5, 6, 7);
  };

  STAMPR(0, 14);
  STAMP(0, 0);
#pragma unroll
  for (int p = 0; p < 10; ++p) stage_piece(0, p);
  if (w >= 4 && w < 7) {
    const int li = ((bm * 32 + j) * 3 + (w - 4)) * 64 + l;
    const bool isd = li < 41472;
    const int ln = isd ? li : (li - 41472 < 5184 ? li - 41472 : 5183);
    touch_line((const char*)(isd ? wd : wo) + (size_t)ln * 128, lds_base + 2 * G1_STAGE);
  }
  STAMP(0, 12);
  int tl = 9;
  asm volatile("" : "+s"(tl));
#pragma clang loop unroll(disable)
  for (int t = 0; t < tl; ++t) {
    asm volatile("s_waitcnt vmcnt(0)" ::: "memory");
    __builtin_amdgcn_s_barrier();
    asm volatile("" ::: "memory");
    STAMP(0, 1 + t);
    const bool issue = t < 8;
    const char* sb = lds + (t & 1) * G1_STAGE;
    bf16x8 xf[2][4], wfa, wfb, wfc;
    wfa = load_w(sb, 0);
#pragma unroll
    for (int rt = 0; rt < 4; ++rt) xf[0][rt] = *(const bf16x8*)(sb + xoff0 + rt * 2048);
    wfb = load_w(sb, 1);
    wfc = load_w(sb, 2);
#pragma unroll
    for (int grp = 0; grp < 22; ++grp) {
      const int kk = grp / 11, i = grp - kk * 11;
      bf16x8 wf = wfa;
      wfa = wfb;
      wfb = wfc;
      if (grp + 3 < 22) wfc = load_w(sb, grp + 3);
      if (grp >= 4 && grp < 8) xf[1][grp - 4] = *(const bf16x8*)(sb + xoff1 + (grp - 4) * 2048);
      if (grp < 10 && issue) stage_piece(t + 1, grp);
      const bool isq = (i == 10) || (WN == 1 && (i == 4 || i == 9));
#pragma unroll
      for (int rt = 0; rt < 4; ++rt) {
        if (ISV && isq) acc[i][rt] = MFMA16(xf[kk][rt], wf, acc[i][rt]);
        else acc[i][rt] = MFMA16(wf, xf[kk][rt], acc[i][rt]);
      }
      __builtin_amdgcn_sched_barrier(0);
    }
  }

  STAMP(0, 10);
  __builtin_amdgcn_sched_barrier(0);
  {
  const int l = lane_id_opaque();
  const int g = l >> 4;
  const int tok0 = bm * 256 + wm * 64;
  constexpr int NP = WN == 0 ? 5 : 4;
#pragma unroll
  for (int i = 0; i < NP; ++i) {
    const int ct = WN * 5 + i;
    const int chunk = j * 18 + ct * 2 + (g >> 1);
#pragma unroll
    for (int rt = 0; rt < 4; ++rt) {
      float hv[4];
#pragma unroll
      for (int r = 0; r < 4; ++r) hv[r] = bf16r(acc[i][rt][r]) * bf16r(acc[i + 5][rt][r]);
      u32x2 o = {pack_bf16(hv[0], hv[1]), pack_bf16(hv[2], hv[3])};
      int token = tok0 + rt * 16 + (l & 15);
      *(u32x2*)(hT + ((size_t)chunk * SEQ + token) * 8 + (g & 1) * 4) = o;
    }
  }
  if (j >= 28) {
    const int tid = w * 64 + l;
    const int token = bm * 256 + (j - 28) * 64 + (tid >> 3), prt = tid & 7;
    const u16* xr = x + (size_t)token * DM + prt * 72;
    const u16* gr = gw + prt * 72;
    bf16x8 xv[9], gv[9];
#pragma unroll
    for (int i = 0; i < 9; ++i) { xv[i] = *(const bf16x8*)(xr + i * 8); gv[i] = *(const bf16x8*)(gr + i * 8); }
    float sum = 0.f;
#pragma unroll
    for (int i = 0; i < 9; ++i)
#pragma unroll
      for (int e = 0; e < 8; ++e) sum += bf16r(bf2f((u16)xv[i][e]) * bf2f((u16)gv[i][e]));
    sum += __shfl_xor(sum, 1);
    sum += __shfl_xor(sum, 2);
    sum += __shfl_xor(sum, 4);
    sum = bf16r(sum);
    if (prt == 0) gout[token] = bf16r(1.0f / (1.0f + __expf(-sum)));
  }
  if (j < 27) {
    if (!ISV) __syncthreads();
    constexpr int NQ = WN == 0 ? 1 : 3;
#pragma unroll
    for (int qi = 0; qi < NQ; ++qi) {
      const int ai = WN == 0 ? 10 : (qi == 0 ? 4 : (qi == 1 ? 9 : 10));
      const int qct = WN == 0 ? 0 : qi + 1;
      if (ISV) {
        int cv = j * 64 + qct * 16 + (l & 15) - 1152;
        int head = cv / 18, d = cv - head * 18;
#pragma unroll
        for (int rt = 0; rt < 4; ++rt) {
          int t16 = tok0 + rt * 16;
          int tile32 = t16 >> 5, s = rt & 1;
          char* dst = KV + (size_t)(head * 64 + tile32) * KVREC + 1024 + ((s * 2 + (g & 1)) * 19 + d) * 16 + (g >> 1) * 8;
          u32x2 o = {pack_bf16(acc[ai][rt][0], acc[ai][rt][1]), pack_bf16(acc[ai][rt][2], acc[ai][rt][3])};
          *(u32x2*)dst = o;
          if (d == 17) *(u32x2*)(dst + 16) = (u32x2){0x3F803F80u, 0x3F803F80u};
        }
      } else {
        const float sc = j < 9 ? 0.23570226039551584f * 1.4426950408889634f : 1.0f;
#pragma unroll
        for (int rt = 0; rt < 4; ++rt) {
          u32x2 o = {pack_bf16(acc[ai][rt][0] * sc, acc[ai][rt][1] * sc), pack_bf16(acc[ai][rt][2] * sc, acc[ai][rt][3] * sc)};
          *(u32x2*)(lds + (wm * 64 + rt * 16 + (l & 15)) * 136 + qct * 32 + g * 8) = o;
        }
      }
    }
    if (!ISV) {
      __syncthreads();
      const bool isQ = j < 9;
      const int tid = w * 64 + l;
      const int pr = tid & 31;
      const int c = j * 64 + 2 * pr - (isQ ? 0 : 576);
      const int head = c / 18, d = c - head * 18;
#pragma unroll 4
      for (int it = 0; it < 16; ++it) {
        const int tl_ = it * 16 + (tid >> 5);
        const unsigned v = *(const unsigned*)(lds + tl_ * 136 + pr * 4);
        const int token = bm * 256 + tl_;
        char* dst;
        if (isQ) dst = (char*)(Qb + ((size_t)head * SEQ + token) * 32 + d);
        else {
          char* rec = KV + (size_t)(head * 64 + (token >> 5)) * KVREC;
          dst = d < 16 ? rec + (token & 31) * 32 + d * 2 : rec + 2240 + (token & 31) * 4;
        }
        *(unsigned*)dst = v;
      }
    }
  }
  }
  STAMP(0, 11);
  STAMPR(0, 15);
}

__global__ __launch_bounds__(512, 2) void gemm1_kernel(const u16* __restrict__ x, const u16* __restrict__ wg,
                                                       const u16* __restrict__ wu, const u16* __restrict__ wqkv,
                                                       u16* __restrict__ hT, u16* __restrict__ Qb,
                                                       char* __restrict__ KV, const u16* __restrict__ gw,
                                                       float* __restrict__ gout, const u16* __restrict__ wd,
                                                       const u16* __restrict__ wo) {
  __shared__ __attribute__((aligned(16))) char lds[2 * G1_STAGE + 256];
  const int b = blockIdx.x;
  const int xcd = b & 7, idx = b >> 3;
  const int j = xcd * 4 + (idx & 3), bm = idx >> 2;
  const int w = __builtin_amdgcn_readfirstlane(threadIdx.x >> 6);
  const bool isv = (j >= 18);
  if (w & 1) {
    if (isv) gemm1_body<true, 1>(x, wg, wu, wqkv, hT, Qb, KV, gw, gout, wd, wo, lds, bm, j, w);
    else gemm1_body<false, 1>(x, wg, wu, wqkv, hT, Qb, KV, gw, gout, wd, wo, lds, bm, j, w);
  } else {
    if (isv) gemm1_body<true, 0>(x, wg, wu, wqkv, hT, Qb, KV, gw, gout, wd, wo, lds, bm, j, w);
    else gemm1_body<false, 0>(x, wg, wu, wqkv, hT, Qb, KV, gw, gout, wd, wo, lds, bm, j, w);
  }
}

DI float xhalf_max(float v) {
  auto rr = __builtin_amdgcn_permlane32_swap(__float_as_uint(v), __float_as_uint(v), false, false);
  return fmaxf(__uint_as_float(rr[0]), __uint_as_float(rr[1]));
}
DI float tile_max(const f32x16& S) {
  float t = fmaxf(fmaxf(S[0], S[1]), S[2]);
#pragma unroll
  for (int i = 3; i < 15; i += 2) t = fmaxf(fmaxf(t, S[i]), S[i + 1]);
  return xhalf_max(fmaxf(t, S[15]));
}
DI f32x16 qk_tile(bf16x8 kA, unsigned kH, bf16x8 qA, u32x4 qB) {
  f32x16 S;
#pragma unroll
  for (int i = 0; i < 16; ++i) S[i] = 0.f;
  u32x4 kBv = {kH, 0x00003F80u, 0u, 0u};
  S = MFMA32(kA, qA, S);
  S = MFMA32(__builtin_bit_cast(bf16x8, kBv), __builtin_bit_cast(bf16x8, qB), S);
  return S;
}
DI void pv_tile(f32x16& S, bf16x8 v0, bf16x8 v1, f32x16& O) {
#pragma unroll
  for (int i = 0; i < 16; ++i) S[i] = __builtin_amdgcn_exp2f(S[i]);
  u32x4 p0 = {pack_bf16(S[0], S[1]), pack_bf16(S[2], S[3]), pack_bf16(S[4], S[5]), pack_bf16(S[6], S[7])};
  u32x4 p1 = {pack_bf16(S[8], S[9]), pack_bf16(S[10], S[11]), pack_bf16(S[12], S[13]), pack_bf16(S[14], S[15])};
  O = MFMA32(v0, __builtin_bit_cast(bf16x8, p0), O);
  O = MFMA32(v1, __builtin_bit_cast(bf16x8, p1), O);
}

constexpr int AT_STAGE = 2 * 8 * KVREC;

DI void attn_body(const u16* __restrict__ Qb, const char* __restrict__ KV, u16* __restrict__ oT, char* lds,
                  const int b) {
  const int head = (b & 7) * 4 + ((b >> 3) & 3), qblk = b >> 5;
  const int w = __builtin_amdgcn_readfirstlane(threadIdx.x >> 6);
  const int l = threadIdx.x & 63, r = l & 31, h = l >> 5;
  const int qp = w & 3, kvh = w >> 2;
  const int token0 = qblk * 256 + qp * 64 + r;

  const unsigned lds_base = (unsigned)(size_t)(LDS_AS char*)lds;
  const char* kvsrc = KV + (size_t)(head * 64) * KVREC;
  auto stage = [&](int s) {
#pragma unroll
    for (int i = 0; i < 5; ++i) {
      int n = w * 5 + i;
      if (n > 37) n -= 2;
      const int hf = n >= 19 ? 1 : 0, pc = n - hf * 19;
      glds16_asm((unsigned)(l * 16), kvsrc + (hf * 32 + s * 8) * KVREC + pc * 1024,
                 lds_base + (s & 1) * AT_STAGE + hf * (8 * KVREC) + pc * 1024);
    }
  };
  STAMPR(1, 14);
  STAMP(1, 0);
  stage(0);

  bf16x8 qA[2];
  u32x4 qB[2];
  float m[2];
  f32x16 O[2];
#pragma unroll
  for (int q = 0; q < 2; ++q) {
    const u16* qrow = Qb + ((size_t)head * SEQ + token0 + q * 32) * 32;
    qA[q] = *(const bf16x8*)(qrow + 8 * h);
    unsigned q67 = *(const unsigned*)(qrow + 16);
    asm volatile("" : "+v"(qA[q]), "+v"(q67));
    qB[q] = (u32x4){h ? 0u : q67, 0u, 0u, 0u};
    m[q] = 0.f;
#pragma unroll
    for (int i = 0; i < 16; ++i) O[q][i] = 0.f;
  }
  bool failed = false;

  const int koff = kvh * (8 * KVREC) + r * 32 + h * 16;
  const int hoff = kvh * (8 * KVREC) + 2240 + r * 4;
  const int voff = kvh * (8 * KVREC) + 1024 + (h * 19 + (r < 18 ? r : 18)) * 16;

#pragma clang loop unroll(disable)
  for (int s = 0; s < 4; ++s) {
    asm volatile("s_waitcnt vmcnt(0)" ::: "memory");
    __builtin_amdgcn_s_barrier();
    asm volatile("" ::: "memory");
    STAMP(1, 1 + s);
    if (s < 3) stage(s + 1);
    const char* sb = lds + (s & 1) * AT_STAGE;
    bf16x8 kA = *(const bf16x8*)(sb + koff);
    unsigned kH = *(const unsigned*)(sb + hoff);
    if (s == 0) {
#pragma unroll
      for (int q = 0; q < 2; ++q) {
        f32x16 S0 = qk_tile(kA, kH, qA[q], qB[q]);
        m[q] = bf16r(tile_max(S0));
        qB[q][1] = h ? 0u : pack_bf16(-m[q], 0.f);
      }
    }
    f32x16 Sa = qk_tile(kA, kH, qA[0], qB[0]);
#pragma unroll
    for (int i = 0; i < 8; ++i) {
      bf16x8 v0 = *(const bf16x8*)(sb + voff + i * KVREC);
      bf16x8 v1 = *(const bf16x8*)(sb + voff + i * KVREC + 608);
      f32x16 Sb = qk_tile(kA, kH, qA[1], qB[1]);
      if (i < 7) {
        kA = *(const bf16x8*)(sb + koff + (i + 1) * KVREC);
        kH = *(const unsigned*)(sb + hoff + (i + 1) * KVREC);
      }
      pv_tile(Sa, v0, v1, O[0]);
      if (i < 7) Sa = qk_tile(kA, kH, qA[0], qB[0]);
      pv_tile(Sb, v0, v1, O[1]);
    }
    float lmax = h ? 0.f : fmaxf(O[0][10], O[1][10]);
    if (__any(!(lmax < 4294967296.f) || !(O[0][10] == O[0][10]) || !(O[1][10] == O[1][10]))) {
#pragma unroll
      for (int q = 0; q < 2; ++q) {
        float lv = O[q][10];
        auto rr = __builtin_amdgcn_permlane32_swap(__float_as_uint(lv), __float_as_uint(lv), false, false);
        lv = __uint_as_float(rr[0]);
        if (__any(!(lv < 1.0e24f))) failed = true;
        float sh = lv > 1.f && lv < 1.0e24f ? floorf(__builtin_amdgcn_logf(lv)) : 0.f;
        float mn = bf16r(m[q] + sh);
        float sc = __builtin_amdgcn_exp2f(m[q] - mn);
        m[q] = mn;
#pragma unroll
        for (int i = 0; i < 16; ++i) O[q][i] *= sc;
        qB[q][1] = h ? 0u : pack_bf16(-mn, 0.f);
      }
    }
  }

  STAMP(1, 5);
  if (failed) {
#pragma clang loop unroll(disable)
    for (int q = 0; q < 2; ++q) {
      const char* rec = KV + (size_t)(head * 64 + kvh * 32) * KVREC;
      const bf16x8 qa = q ? qA[1] : qA[0];
      u32x4 qb = q ? qB[1] : qB[0];
      qb[1] = 0u;
      float mm = -3.0e38f;
      f32x16 Oo;
#pragma unroll
      for (int i = 0; i < 16; ++i) Oo[i] = 0.f;
#pragma clang loop unroll(disable)
      for (int it = 0; it < 32; ++it, rec += KVREC) {
        bf16x8 kA = *(const bf16x8*)(rec + r * 32 + h * 16);
        unsigned kH = *(const unsigned*)(rec + 2240 + r * 4);
        bf16x8 v0 = *(const bf16x8*)(rec + 1024 + (h * 19 + (r < 18 ? r : 18)) * 16);
        bf16x8 v1 = *(const bf16x8*)(rec + 1024 + 608 + (h * 19 + (r < 18 ? r : 18)) * 16);
        f32x16 S = qk_tile(kA, kH, qa, qb);
        float mn = fmaxf(mm, tile_max(S));
        float sc = __builtin_amdgcn_exp2f(mm - mn);
        mm = mn;
#pragma unroll
        for (int i = 0; i < 16; ++i) { S[i] -= mn; Oo[i] *= sc; }
        pv_tile(S, v0, v1, Oo);
      }
      if (q) { O[1] = Oo; m[1] = mm; } else { O[0] = Oo; m[0] = mm; }
    }
  }

  __syncthreads();
  float* mrg = (float*)lds;
  if (kvh == 1) {
#pragma unroll
    for (int q = 0; q < 2; ++q) {
#pragma unroll
      for (int i = 0; i < 12; ++i) mrg[((qp * 2 + q) * 13 + i) * 64 + l] = O[q][i];
      mrg[((qp * 2 + q) * 13 + 12) * 64 + l] = m[q];
    }
  }
  __syncthreads();
  if (kvh == 0) {
#pragma unroll
    for (int q = 0; q < 2; ++q) {
      float m1 = mrg[((qp * 2 + q) * 13 + 12) * 64 + l];
      float mm = fmaxf(m[q], m1);
      float a0 = __builtin_amdgcn_exp2f(m[q] - mm), a1 = __builtin_amdgcn_exp2f(m1 - mm);
      float o[12];
#pragma unroll
      for (int i = 0; i < 12; ++i) o[i] = O[q][i] * a0 + mrg[((qp * 2 + q) * 13 + i) * 64 + l] * a1;
      float lsum = __shfl(o[10], r);
      float inv = 1.0f / lsum;
      const int token = token0 + q * 32;
#pragma unroll
      for (int pr = 0; pr < 5; ++pr) {
        if (pr == 4 && h) continue;
        int d = (pr >> 1) * 8 + (pr & 1) * 2 + 4 * h;
        int c = head * 18 + d;
        unsigned v = pack_bf16(o[2 * pr] * inv, o[2 * pr + 1] * inv);
        *(unsigned*)(oT + ((size_t)(c >> 3) * SEQ + token) * 8 + (c & 7)) = v;
      }
    }
  }
  STAMP(1, 6);
  STAMPR(1, 15);
}

template <int N> DI void wait_vm() { asm volatile("s_waitcnt vmcnt(%0)" ::"i"(N) : "memory"); }
DI void raw_barrier() {
  __builtin_amdgcn_s_barrier();
  asm volatile("" ::: "memory");
}

template <int NW, int RT, int NS = 4>
struct Gemm2 {
  static constexpr int BM = NW * RT * 16;
  static constexpr int AB = 8 * BM * 16;
  static constexpr int STG = AB + 18432;
  static constexpr int NAW = AB / 1024 / NW;
  static constexpr int NWW = (18 + NW - 1) / NW;
  static constexpr int NV = NAW + NWW;
  static constexpr int LA = 4;

  const char* abase;
  const char* wbase;
  unsigned avo[NAW], wvo[NWW];
  unsigned lds_base;
  char* lds;
  int w, aoff, woff0, woff1;
  bf16x8 af[2][RT], wq_[LA];

  DI void init(const u16* AT, const u16* W, int tok0, int col0, int kbeg, char* lds_, int w_, int l) {
    lds = lds_; w = w_;
    lds_base = (unsigned)(size_t)(LDS_AS char*)lds_;
    abase = (const char*)(AT + (size_t)(kbeg / 8) * SEQ * 8);
    wbase = (const char*)(W + (size_t)kbeg * DM);
    const int g = l >> 4;
#pragma unroll
    for (int i = 0; i < NAW; ++i) {
      int off = (w * NAW + i) * 64 + l;
      int chunk = off / BM, token = off - chunk * BM;
      avo[i] = (unsigned)((chunk * SEQ + tok0 + token) * 16);
    }
#pragma unroll
    for (int i = 0; i < NWW; ++i) {
      int n = w + NW * i;
      if (n >= 18) n = 16 + (n & 1);
      int c = n * 64 + l;
      int lr = c / 18, ch = c - lr * 18;
      int krow = lr ^ ((lr & 8) >> 1);
      wvo[i] = (unsigned)((krow * DM + col0 + ch * 8) * 2);
    }
    aoff = g * (BM * 16) + (w * RT * 16 + (l & 15)) * 16;
    const int wq = (l & 15) >> 2, wp = l & 3;
    woff0 = AB + (g * 8 + 4 * (0 ^ (g & 1)) + wq) * 288 + wp * 8;
    woff1 = AB + (g * 8 + 4 * (1 ^ (g & 1)) + wq) * 288 + wp * 8;
  }
  DI void piece(int t, int p) {
    const unsigned sb = lds_base + (t & (NS - 1)) * STG;
    if (p < NAW) {
      glds16_asm(avo[p], abase + (size_t)t * (8 * SEQ * 16), sb + (w * NAW + p) * 1024);
    } else {
      const int i = p - NAW;
      int n = w + NW * i;
      if (n >= 18) n = 16 + (n & 1);
      glds16_asm(wvo[i], wbase + (size_t)t * (64 * DM * 2), sb + AB + n * 1024);
    }
  }
  DI bf16x8 load_w(const char* sb, int grp) {
    const int kk = grp / 9, ct = grp - kk * 9;
    s16x4 lo = tr_read(sb + woff0 + kk * 9216 + ct * 32);
    s16x4 hi = tr_read(sb + woff1 + kk * 9216 + ct * 32);
    return __builtin_shufflevector(lo, hi, 0, 1, 2, 3, 4, 5, 6, 7);
  }
  DI bf16x8 load_a(const char* sb, int kk, int rt) {
    return *(const bf16x8*)(sb + aoff + kk * 4 * BM * 16 + rt * 256);
  }
  DI void prologue_issue() {
#pragma unroll
    for (int t = 0; t < 3; ++t)
#pragma unroll
      for (int p = 0; p < NV; ++p) piece(t, p);
  }
  DI void prologue_read() {
    wait_vm<2 * NV>();
    raw_barrier();
#pragma unroll
    for (int kk = 0; kk < 2; ++kk)
#pragma unroll
      for (int rt = 0; rt < RT; ++rt) af[kk][rt] = load_a(lds, kk, rt);
#pragma unroll
    for (int i = 0; i < LA; ++i) wq_[i] = load_w(lds, i);
  }
  template <bool ISSUE, bool PREF>
  DI void body(int t, f32x4 (&acc)[9][RT]) {
    const char* sb = lds + (t & 3) * STG;
    const char* sn = lds + ((t + 1) & 3) * STG;
    bf16x8 an[2][RT];
#pragma unroll
    for (int grp = 0; grp < 18; ++grp) {
      bf16x8 wf = wq_[0];
#pragma unroll
      for (int i = 0; i + 1 < LA; ++i) wq_[i] = wq_[i + 1];
      if (grp + LA < 18) wq_[LA - 1] = load_w(sb, grp + LA);
      else if (PREF) wq_[LA - 1] = load_w(sn, grp + LA - 18);
      if (PREF && grp >= 8 && grp < 8 + 2 * RT) an[(grp - 8) / RT][(grp - 8) % RT] = load_a(sn, (grp - 8) / RT, (grp - 8) % RT);
      if (ISSUE && grp < NV) piece(t + 3, grp);
      const int kk = grp / 9, ct = grp - kk * 9;
#pragma unroll
      for (int rt = 0; rt < RT; ++rt) acc[ct][rt] = MFMA16(wf, af[kk][rt], acc[ct][rt]);
      __builtin_amdgcn_sched_barrier(0);
    }
    if (PREF) {
#pragma unroll
      for (int kk = 0; kk < 2; ++kk)
#pragma unroll
        for (int rt = 0; rt < RT; ++rt) af[kk][rt] = an[kk][rt];
    }
  }
  template <bool ISSUE>
  DI void body2(int t, f32x4 (&acc)[9][RT]) {
    const char* sb = lds + (t & 1) * STG;
#pragma unroll
    for (int kk = 0; kk < 2; ++kk)
#pragma unroll
      for (int rt = 0; rt < RT; ++rt) af[kk][rt] = load_a(sb, kk, rt);
#pragma unroll
    for (int i = 0; i < LA; ++i) wq_[i] = load_w(sb, i);
#pragma unroll
    for (int grp = 0; grp < 18; ++grp) {
      bf16x8 wf = wq_[0];
#pragma unroll
      for (int i = 0; i + 1 < LA; ++i) wq_[i] = wq_[i + 1];
      if (grp + LA < 18) wq_[LA - 1] = load_w(sb, grp + LA);
      if (ISSUE && grp < NV) piece(t + 1, grp);
      const int kk = grp / 9, ct = grp - kk * 9;
#pragma unroll
      for (int rt = 0; rt < RT; ++rt) acc[ct][rt] = MFMA16(wf, af[kk][rt], acc[ct][rt]);
      __builtin_amdgcn_sched_barrier(0);
    }
  }
  DI void run2(int nsteps, f32x4 (&acc)[9][RT]) {
    int tl = nsteps - 1;
    asm volatile("" : "+s"(tl));
#pragma clang loop unroll(disable)
    for (int t = 0; t < tl; ++t) {
      wait_vm<0>();
      raw_barrier();
      body2<true>(t, acc);
    }
    wait_vm<0>();
    raw_barrier();
    body2<false>(tl, acc);
  }
  DI void run(int nsteps, f32x4 (&acc)[9][RT]) {
    prologue_read();
    int tl = nsteps - 3;
    asm volatile("" : "+s"(tl));
#pragma clang loop unroll(disable)
    for (int t = 0; t < tl; ++t) {
      wait_vm<NV>();
      raw_barrier();
      body<true, true>(t, acc);
    }
    wait_vm<NV>();
    raw_barrier();
    body<false, true>(tl, acc);
    wait_vm<0>();
    raw_barrier();
    body<false, true>(tl + 1, acc);
    body<false, false>(tl + 2, acc);
  }
};

DI void down_body(const u16* __restrict__ hT, const u16* __restrict__ wd, u16* __restrict__ part, char* lds,
                  const int b) {
  using G = Gemm2<8, 1, 2>;
  const int xcd = b & 7, idx = b >> 3;
  const int ks = xcd >> 1;
  const int bm = (xcd & 1) * 8 + (idx >> 2);
  const int bn = idx & 3;
  const int w = __builtin_amdgcn_readfirstlane(threadIdx.x >> 6);
  const int l = threadIdx.x & 63;
  STAMPR(2, 14);
  STAMP(2, 0);
  G gm;
  gm.init(hT, wd, bm * 128, bn * 144, ks * 1152, lds, w, l);
#pragma unroll
  for (int p = 0; p < G::NV; ++p) gm.piece(0, p);
  STAMP(2, 1);
  f32x4 acc[9][1];
#pragma unroll
  for (int i = 0; i < 9; ++i) acc[i][0] = (f32x4){0.f, 0.f, 0.f, 0.f};
  gm.run2(18, acc);
  STAMP(2, 2);
#pragma unroll
  for (int ct = 0; ct < 9; ++ct) {
    int tt = bm * 8 + w, ctg = bn * 9 + ct;
    u32x2 pv = {pack_bf16(acc[ct][0][0], acc[ct][0][1]), pack_bf16(acc[ct][0][2], acc[ct][0][3])};
    *(u32x2*)(part + ((size_t)(ks * 128 + tt) * 36 + ctg) * 256 + l * 4) = pv;
  }
  STAMP(2, 3);
  STAMPR(2, 15);
}

__global__ __launch_bounds__(512, 4) void mid_kernel(const u16* __restrict__ Qb, const char* __restrict__ KV,
                                                     u16* __restrict__ oT, const u16* __restrict__ hT,
                                                     const u16* __restrict__ wd, u16* __restrict__ part) {
  __shared__ __attribute__((aligned(16))) char lds[2 * AT_STAGE];
  const int b = blockIdx.x;
  if (b >= 256) down_body(hT, wd, part, lds, b - 256);
  else attn_body(Qb, KV, oT, lds, b);
}

__global__ __launch_bounds__(256, 2) void final_kernel(const u16* __restrict__ oT, const u16* __restrict__ wo,
                                                       const u16* __restrict__ part, const float* __restrict__ gin,
                                                       u16* __restrict__ out) {
  __shared__ __attribute__((aligned(16))) char lds[576 * 96 + 2048];
  const int b = blockIdx.x;
  const int idx = b >> 3;
  const int bm = (b & 7) * 4 + idx / 12, bn = idx % 12;
  const int w = __builtin_amdgcn_readfirstlane(threadIdx.x >> 6);
  const int l = threadIdx.x & 63, g = l >> 4;
  STAMPR(3, 14);
  STAMP(3, 0);
  const unsigned lds_base = (unsigned)(size_t)(LDS_AS char*)lds;
#pragma unroll
  for (int i = 0; i < 14; ++i) {
    const int n = w * 14 + i;
    int c = n * 64 + l;
    int lr = c / 6, ch = c - lr * 6;
    if (lr > 575) lr = 575;
    int krow = lr ^ ((lr & 8) >> 1);
    glds16_asm((unsigned)((krow * DM + bn * 48 + ch * 8) * 2), wo, lds_base + n * 1024);
  }
  const int token = bm * 64 + w * 16 + (l & 15);
  bf16x8 af[18];
#pragma unroll
  for (int ks = 0; ks < 18; ++ks) af[ks] = *(const bf16x8*)(oT + ((size_t)(ks * 4 + g) * SEQ + token) * 8);
  float gg = gin[token];
  const int tt = bm * 4 + w;
  f32x4 bsum[3];
#pragma unroll
  for (int ct = 0; ct < 3; ++ct) {
    int ctg = bn * 3 + ct;
    f32x4 pp[4];
#pragma unroll
    for (int ks = 0; ks < 4; ++ks) {
      u32x2 v = *(const u32x2*)(part + ((size_t)(ks * 128 + tt) * 36 + ctg) * 256 + l * 4);
      pp[ks] = (f32x4){__uint_as_float(v[0] << 16), __uint_as_float(v[0] & 0xffff0000u),
                       __uint_as_float(v[1] << 16), __uint_as_float(v[1] & 0xffff0000u)};
    }
    bsum[ct] = (pp[0] + pp[1]) + (pp[2] + pp[3]);
  }
  STAMP(3, 1);
  asm volatile("s_waitcnt vmcnt(0)" ::: "memory");
  __builtin_amdgcn_s_barrier();
  asm volatile("" ::: "memory");
  STAMP(3, 2);
  const int wq = (l & 15) >> 2, wp = l & 3;
  const int woff0 = (g * 8 + 4 * (0 ^ (g & 1)) + wq) * 96 + wp * 8;
  const int woff1 = (g * 8 + 4 * (1 ^ (g & 1)) + wq) * 96 + wp * 8;
  f32x4 acc[3];
#pragma unroll
  for (int i = 0; i < 3; ++i) acc[i] = (f32x4){0.f, 0.f, 0.f, 0.f};
#pragma unroll
  for (int ks = 0; ks < 18; ++ks)
#pragma unroll
    for (int ct = 0; ct < 3; ++ct) {
      s16x4 lo = tr_read(lds + woff0 + ks * 3072 + ct * 32);
      s16x4 hi = tr_read(lds + woff1 + ks * 3072 + ct * 32);
      bf16x8 wf = __builtin_shufflevector(lo, hi, 0, 1, 2, 3, 4, 5, 6, 7);
      acc[ct] = MFMA16(wf, af[ks], acc[ct]);
    }
  STAMP(3, 3);
  const float og = bf16r(1.0f - gg);
#pragma unroll
  for (int ct = 0; ct < 3; ++ct) {
    int ctg = bn * 3 + ct;
    float o[4];
#pragma unroll
    for (int e = 0; e < 4; ++e) o[e] = bf16r(gg * bf16r(acc[ct][e])) + bf16r(og * bf16r(bsum[ct][e]));
    u32x2 ov = {pack_bf16(o[0], o[1]), pack_bf16(o[2], o[3])};
    *(u32x2*)(out + (size_t)token * DM + ctg * 16 + g * 4) = ov;
  }
  STAMP(3, 4);
  STAMPR(3, 15);
}

extern "C" void kernel_launch(void* const* d_in, const int* in_sizes, int n_in, void* d_out, int out_size,
                              void* d_ws, size_t ws_size, hipStream_t stream) {
  const u16* x = (const u16*)d_in[0];
  const u16* wqkv = (const u16*)d_in[1];
  const u16* wo = (const u16*)d_in[2];
  const u16* wg = (const u16*)d_in[4];
  const u16* wu = (const u16*)d_in[5];
  const u16* wd = (const u16*)d_in[6];
  const u16* gw = (const u16*)d_in[7];
  char* ws = (char*)d_ws;
  u16* hT = (u16*)(ws + WS_HT);
  u16* Qb = (u16*)(ws + WS_QB);
  char* KV = ws + WS_KV;
  u16* oT = (u16*)(ws + WS_OT);
  u16* part = (u16*)(ws + WS_PART);
  float* gbuf = (float*)(ws + WS_G);
  u16* out = (u16*)d_out;

  gemm1_kernel<<<256, 512, 0, stream>>>(x, wg, wu, wqkv, hT, Qb, KV, gw, gbuf, wd, wo);
  mid_kernel<<<512, 512, 0, stream>>>(Qb, KV, oT, hT, wd, part);
  final_kernel<<<384, 256, 0, stream>>>(oT, wo, part, gbuf, out);
}
```

```cpp
#include <hip/hip_runtime.h>
#include <hip/hip_bf16.h>
#include <stdint.h>
#include <type_traits>

typedef unsigned short u16;
typedef __attribute__((ext_vector_type(8))) short bf16x8;
typedef __attribute__((ext_vector_type(4))) short s16x4;
typedef __attribute__((ext_vector_type(4))) float f32x4;
typedef __attribute__((ext_vector_type(16))) float f32x16;
typedef __attribute__((ext_vector_type(2))) float f32x2;
typedef __attribute__((ext_vector_type(2))) __bf16 bf16x2_t;
typedef __attribute__((ext_vector_type(2))) unsigned u32x2;
typedef __attribute__((ext_vector_type(4))) unsigned u32x4;

#define DI __device__ __forceinline__
#define LDS_AS __attribute__((address_space(3)))

constexpr int SEQ = 2048, DM = 576, FF = 4608, NH = 32, DH = 18, NQKV = 1728;

DI unsigned pack_bf16(float lo, float hi) {
  f32x2 f = {lo, hi};
  bf16x2_t b = __builtin_convertvector(f, bf16x2_t);
  return __builtin_bit_cast(unsigned, b);
}
DI float bf16r(float x) { return __uint_as_float(pack_bf16(x, x) << 16); }
DI float bf2f(u16 v) { return __uint_as_float(((unsigned)v) << 16); }

DI s16x4 tr_read(const char* lds_ptr) {
  return __builtin_amdgcn_ds_read_tr16_b64_v4i16((LDS_AS s16x4*)(lds_ptr));
}
DI void glds16(const void* gsrc, char* lds_dst) {
  __builtin_amdgcn_global_load_lds((const __attribute__((address_space(1))) void*)gsrc,
                                   (LDS_AS void*)lds_dst, 16, 0, 0);
}
DI int lane_id_opaque() {
  int l;
  asm volatile("v_mbcnt_lo_u32_b32 %0, -1, 0\n\tv_mbcnt_hi_u32_b32 %0, -1, %0" : "=v"(l));
  return l;
}
DI void glds16_asm(unsigned voff, const void* sbase, unsigned lds_dst) {
  unsigned keep;
  asm volatile("s_mov_b32 %0, m0\n\ts_mov_b32 m0, %3\n\ts_nop 0\n\tglobal_load_lds_dwordx4 %1, %2\n\ts_mov_b32 m0, %0"
               : "=&s"(keep) : "v"(voff), "s"(sbase), "s"(lds_dst) : "memory");
}
DI void touch_line(const void* gptr, unsigned lds_scratch) {
  unsigned keep;
  asm volatile("s_mov_b32 %0, m0\n\ts_mov_b32 m0, %2\n\ts_nop 0\n\tglobal_load_lds_dword %1, off\n\ts_mov_b32 m0, %0"
               : "=&s"(keep) : "v"(gptr), "s"(lds_scratch) : "memory");
}
DI void touch_line_s(unsigned voff, const void* sbase, unsigned lds_scratch) {
  unsigned keep;
  asm volatile("s_mov_b32 %0, m0\n\ts_mov_b32 m0, %3\n\ts_nop 0\n\tglobal_load_lds_dword %1, %2\n\ts_mov_b32 m0, %0"
               : "=&s"(keep) : "v"(voff), "s"(sbase), "s"(lds_scratch) : "memory");
}
#define STAMP(kid, i) do {} while (0)
#define STAMPR(kid, i) do {} while (0)
#define MFMA16(a, b, c) __builtin_amdgcn_mfma_f32_16x16x32_bf16((a), (b), (c), 0, 0, 0)
#define MFMA32(a, b, c) __builtin_amdgcn_mfma_f32_32x32x16_bf16((a), (b), (c), 0, 0, 0)

constexpr size_t WS_HT = 0;
constexpr size_t WS_QB = WS_HT + (size_t)FF * SEQ * 2;
constexpr int KVREC = 2432;
constexpr size_t WS_KV = WS_QB + (size_t)NH * SEQ * 32 * 2;
constexpr size_t WS_VF = WS_KV;
constexpr size_t WS_OT = WS_KV + (size_t)NH * 64 * KVREC;
constexpr size_t WS_PART = WS_OT + (size_t)DM * SEQ * 2;
constexpr size_t WS_G = WS_PART + (size_t)4 * SEQ * DM * 4;
constexpr size_t WS_END = WS_G + SEQ * 4;

constexpr int G1_XB = 256 * 128;
constexpr int G1_WB = 18432 * 2 + 10240;
constexpr int G1_STAGE = G1_XB + G1_WB;

template <bool ISV, int WN>
DI void gemm1_body(const u16* __restrict__ x, const u16* __restrict__ wg, const u16* __restrict__ wu,
                   const u16* __restrict__ wqkv, u16* __restrict__ hT, u16* __restrict__ Qb,
                   char* __restrict__ KV, const u16* __restrict__ gw, float* __restrict__ gout, const u16* wd, const u16* wo, char* lds,
                   int bm, int j, int w) {
  const int l = lane_id_opaque();
  const int wm = w >> 1;
  const int g = l >> 4;
  const int jq = j < 27 ? j : 26;

  unsigned xvo, guvo[2], evo[2];
  const char* ebase[2];
  int estep, edst[2];
  {
    int row = 32 * w + (l >> 3);
    int chunk = (l & 7) ^ (l >> 3);
    xvo = (unsigned)((bm * 256 + row) * DM + chunk * 8) * 2u;
#pragma unroll
    for (int i = 0; i < 2; ++i) {
      int c = (w + 8 * i) * 64 + l;
      int lr = c / 18, ch = c - lr * 18;
      int krow = lr ^ ((lr & 8) >> 1);
      guvo[i] = (unsigned)(krow * FF + j * 144 + ch * 8) * 2u;
    }
    if (w < 2) {
      int c = (16 + w) * 64 + l;
      int lr = c / 18, ch = c - lr * 18;
      int krow = lr ^ ((lr & 8) >> 1);
      evo[0] = evo[1] = (unsigned)(krow * FF + j * 144 + ch * 8) * 2u;
      ebase[0] = (const char*)wg; ebase[1] = (const char*)wu;
      estep = 64 * FF * 2;
      edst[0] = G1_XB + (16 + w) * 1024; edst[1] = G1_XB + 18432 + (16 + w) * 1024;
    } else {
#pragma unroll
      for (int i = 0; i < 2; ++i) {
        int n = (w - 2) + 6 * i;
        if (n > 9) n = 9;
        int c = n * 64 + l;
        int lr = c / 10, ch = c - lr * 10;
        if (ch > 7) ch = 7;
        int krow = lr ^ ((lr & 8) >> 1);
        evo[i] = (unsigned)(krow * NQKV + jq * 64 + ch * 8) * 2u;
        ebase[i] = (const char*)wqkv;
        edst[i] = G1_XB + 36864 + n * 1024;
      }
      estep = 64 * NQKV * 2;
    }
    edst[0] = __builtin_amdgcn_readfirstlane(edst[0]);
    edst[1] = __builtin_amdgcn_readfirstlane(edst[1]);
  }

  const unsigned lds_base = (unsigned)(size_t)(LDS_AS char*)lds;
  auto stage_piece = [&](int t, int p) {
    const unsigned sb = lds_base + (t & 1) * G1_STAGE;
    if (p < 4) {
      const char* xb = (const char*)x + t * 128 + p * 8 * DM * 2;
      glds16_asm(xvo, xb, sb + (32 * w + 8 * p) * 128);
    } else if (p < 8) {
      const int i = (p - 4) >> 1;
      const char* gb = (const char*)((p & 1) ? wu : wg) + (size_t)t * (64 * FF * 2);
      glds16_asm(guvo[i], gb, sb + G1_XB + (p & 1) * 18432 + (w + 8 * i) * 1024);
    } else {
      glds16_asm(evo[p & 1], ebase[p & 1] + (size_t)t * estep, sb + edst[p & 1]);
    }
  };

  f32x4 acc[11][4];
#pragma unroll
  for (int i = 0; i < 11; ++i)
#pragma unroll
    for (int r = 0; r < 4; ++r) acc[i][r] = (f32x4){0.f, 0.f, 0.f, 0.f};

  const int xoff0 = wm * 8192 + (l & 15) * 128 + (((0 + g) ^ (l & 7)) << 4);
  const int xoff1 = wm * 8192 + (l & 15) * 128 + (((4 + g) ^ (l & 7)) << 4);
  const int wq = (l & 15) >> 2, wp = l & 3;
  const int lr0 = g * 8 + 4 * (0 ^ (g & 1)) + wq, lr1 = g * 8 + 4 * (1 ^ (g & 1)) + wq;
  const int woff0 = G1_XB + lr0 * 288 + wp * 8, woff1 = G1_XB + lr1 * 288 + wp * 8;
  const int qoff0 = G1_XB + 36864 + lr0 * 160 + wp * 8, qoff1 = G1_XB + 36864 + lr1 * 160 + wp * 8;

  auto load_w = [&](const char* sb, int grp) -> bf16x8 {
    const int kk = grp / 11, i = grp - kk * 11;
    const bool isq = (i == 10) || (WN == 1 && (i == 4 || i == 9));
    s16x4 lo, hi;
    if (isq) {
      const int qct = WN == 0 ? 0 : (i == 4 ? 1 : (i == 9 ? 2 : 3));
      lo = tr_read(sb + qoff0 + kk * 5120 + qct * 32);
      hi = tr_read(sb + qoff1 + kk * 5120 + qct * 32);
    } else {
      const int off = (i < 5 ? (WN * 5 + i) * 32 : 18432 + (WN * 5 + i - 5) * 32) + kk * 9216;
      lo = tr_read(sb + woff0 + off);
      hi = tr_read(sb + woff1 + off);
    }
    return __builtin_shufflevector(lo, hi, 0, 1, 2, 3, 4, 5, 6, 7);
  };

  STAMPR(0, 14);
  STAMP(0, 0);
#pragma unroll
  for (int p = 0; p < 10; ++p) stage_piece(0, p);
#pragma unroll
  for (int p = 0; p < 10; ++p) stage_piece(1, p);
  if (w >= 4 && w < 7) {
    const int li = ((bm * 32 + j) * 3 + (w - 4)) * 64 + l;
    const bool isd = li < 41472;
    const int ln = isd ? li : (li - 41472 < 5184 ? li - 41472 : 5183);
    touch_line((const char*)(isd ? wd : wo) + (size_t)ln * 128, lds_base + 2 * G1_STAGE);
  }
  STAMP(0, 12);
  int tl = 9;
  asm volatile("" : "+s"(tl));
#pragma clang loop unroll(disable)
  for (int t = 0; t < tl; ++t) {
    if (t == 0) asm volatile("s_waitcnt vmcnt(10)" ::: "memory");
    else asm volatile("s_waitcnt vmcnt(0)" ::: "memory");
    __builtin_amdgcn_s_barrier();
    asm volatile("" ::: "memory");
    STAMP(0, 1 + t);
    const bool issue = t >= 1 && t < 8;
    const char* sb = lds + (t & 1) * G1_STAGE;
    bf16x8 xf[2][4], wfa, wfb, wfc;
    wfa = load_w(sb, 0);
#pragma unroll
    for (int rt = 0; rt < 4; ++rt) xf[0][rt] = *(const bf16x8*)(sb + xoff0 + rt * 2048);
    wfb = load_w(sb, 1);
    wfc = load_w(sb, 2);
#pragma unroll
    for (int grp = 0; grp < 22; ++grp) {
      const int kk = grp / 11, i = grp - kk * 11;
      bf16x8 wf = wfa;
      wfa = wfb;
      wfb = wfc;
      if (grp + 3 < 22) wfc = load_w(sb, grp + 3);
      if (grp >= 4 && grp < 8) xf[1][grp - 4] = *(const bf16x8*)(sb + xoff1 + (grp - 4) * 2048);
      if (grp < 10 && issue) stage_piece(t + 1, grp);
      const bool isq = (i == 10) || (WN == 1 && (i == 4 || i == 9));
#pragma unroll
      for (int rt = 0; rt < 4; ++rt) {
        if (ISV && isq) acc[i][rt] = MFMA16(xf[kk][rt], wf, acc[i][rt]);
        else acc[i][rt] = MFMA16(wf, xf[kk][rt], acc[i][rt]);
      }
      __builtin_amdgcn_sched_barrier(0);
    }
  }

  STAMP(0, 10);
  __builtin_amdgcn_sched_barrier(0);
  {
  const int l = lane_id_opaque();
  const int g = l >> 4;
  const int tok0 = bm * 256 + wm * 64;
  constexpr int NP = WN == 0 ? 5 : 4;
#pragma unroll
  for (int i = 0; i < NP; ++i) {
    const int ct = WN * 5 + i;
    const int chunk = j * 18 + ct * 2 + (g >> 1);
#pragma unroll
    for (int rt = 0; rt < 4; ++rt) {
      float hv[4];
#pragma unroll
      for (int r = 0; r < 4; ++r) hv[r] = bf16r(acc[i][rt][r]) * bf16r(acc[i + 5][rt][r]);
      u32x2 o = {pack_bf16(hv[0], hv[1]), pack_bf16(hv[2], hv[3])};
      int token = tok0 + rt * 16 + (l & 15);
      asm volatile("global_store_dwordx2 %0, %1, off sc1" ::"v"(hT + ((size_t)chunk * SEQ + token) * 8 + (g & 1) * 4), "v"(o) : "memory");
    }
  }
  if (j >= 28) {
    const int tid = w * 64 + l;
    const int token = bm * 256 + (j - 28) * 64 + (tid >> 3), prt = tid & 7;
    const u16* xr = x + (size_t)token * DM + prt * 72;
    const u16* gr = gw + prt * 72;
    bf16x8 xv[9], gv[9];
#pragma unroll
    for (int i = 0; i < 9; ++i) { xv[i] = *(const bf16x8*)(xr + i * 8); gv[i] = *(const bf16x8*)(gr + i * 8); }
    float sum = 0.f;
#pragma unroll
    for (int i = 0; i < 9; ++i)
#pragma unroll
      for (int e = 0; e < 8; ++e) sum += bf16r(bf2f((u16)xv[i][e]) * bf2f((u16)gv[i][e]));
    sum += __shfl_xor(sum, 1);
    sum += __shfl_xor(sum, 2);
    sum += __shfl_xor(sum, 4);
    sum = bf16r(sum);
    if (prt == 0) gout[token] = bf16r(1.0f / (1.0f + __expf(-sum)));
  }
  if (j < 27) {
    if (!ISV) __syncthreads();
    constexpr int NQ = WN == 0 ? 1 : 3;
#pragma unroll
    for (int qi = 0; qi < NQ; ++qi) {
      const int ai = WN == 0 ? 10 : (qi == 0 ? 4 : (qi == 1 ? 9 : 10));
      const int qct = WN == 0 ? 0 : qi + 1;
      if (ISV) {
        int cv = j * 64 + qct * 16 + (l & 15) - 1152;
        int head = cv / 18, d = cv - head * 18;
#pragma unroll
        for (int rt = 0; rt < 4; ++rt) {
          int t16 = tok0 + rt * 16;
          int tile32 = t16 >> 5, s = rt & 1;
          char* dst = KV + (size_t)(head * 64 + tile32) * KVREC + 1024 + ((s * 2 + (g & 1)) * 19 + d) * 16 + (g >> 1) * 8;
          u32x2 o = {pack_bf16(acc[ai][rt][0], acc[ai][rt][1]), pack_bf16(acc[ai][rt][2], acc[ai][rt][3])};
          *(u32x2*)dst = o;
          if (d == 17) *(u32x2*)(dst + 16) = (u32x2){0x3F803F80u, 0x3F803F80u};
        }
      } else {
        const float sc = j < 9 ? 0.23570226039551584f * 1.4426950408889634f : 1.0f;
#pragma unroll
        for (int rt = 0; rt < 4; ++rt) {
          u32x2 o = {pack_bf16(acc[ai][rt][0] * sc, acc[ai][rt][1] * sc), pack_bf16(acc[ai][rt][2] * sc, acc[ai][rt][3] * sc)};
          *(u32x2*)(lds + (wm * 64 + rt * 16 + (l & 15)) * 136 + qct * 32 + g * 8) = o;
        }
      }
    }
    if (!ISV) {
      __syncthreads();
      const bool isQ = j < 9;
      const int tid = w * 64 + l;
      const int pr = tid & 31;
      const int c = j * 64 + 2 * pr - (isQ ? 0 : 576);
      const int head = c / 18, d = c - head * 18;
#pragma unroll 4
      for (int it = 0; it < 16; ++it) {
        const int tl_ = it * 16 + (tid >> 5);
        const unsigned v = *(const unsigned*)(lds + tl_ * 136 + pr * 4);
        const int token = bm * 256 + tl_;
        char* dst;
        if (isQ) dst = (char*)(Qb + ((size_t)head * SEQ + token) * 32 + d);
        else {
          char* rec = KV + (size_t)(head * 64 + (token >> 5)) * KVREC;
          dst = d < 16 ? rec + (token & 31) * 32 + d * 2 : rec + 2240 + (token & 31) * 4;
        }
        *(unsigned*)dst = v;
      }
    }
  }
  }
  STAMP(0, 11);
  STAMPR(0, 15);
}

__global__ __launch_bounds__(512, 2) void gemm1_kernel(const u16* __restrict__ x, const u16* __restrict__ wg,
                                                       const u16* __restrict__ wu, const u16* __restrict__ wqkv,
                                                       u16* __restrict__ hT, u16* __restrict__ Qb,
                                                       char* __restrict__ KV, const u16* __restrict__ gw,
                                                       float* __restrict__ gout, const u16* __restrict__ wd,
                                                       const u16* __restrict__ wo) {
  __shared__ __attribute__((aligned(16))) char lds[2 * G1_STAGE + 256];
  const int b = blockIdx.x;
  const int xcd = b & 7, idx = b >> 3;
  const int j = xcd * 4 + (idx & 3), bm = idx >> 2;
  const int w = __builtin_amdgcn_readfirstlane(threadIdx.x >> 6);
  const bool isv = (j >= 18);
  if (w & 1) {
    if (isv) gemm1_body<true, 1>(x, wg, wu, wqkv, hT, Qb, KV, gw, gout, wd, wo, lds, bm, j, w);
    else gemm1_body<false, 1>(x, wg, wu, wqkv, hT, Qb, KV, gw, gout, wd, wo, lds, bm, j, w);
  } else {
    if (isv) gemm1_body<true, 0>(x, wg, wu, wqkv, hT, Qb, KV, gw, gout, wd, wo, lds, bm, j, w);
    else gemm1_body<false, 0>(x, wg, wu, wqkv, hT, Qb, KV, gw, gout, wd, wo, lds, bm, j, w);
  }
}

DI float xhalf_max(float v) {
  auto rr = __builtin_amdgcn_permlane32_swap(__float_as_uint(v), __float_as_uint(v), false, false);
  return fmaxf(__uint_as_float(rr[0]), __uint_as_float(rr[1]));
}
DI float tile_max(const f32x16& S) {
  float t = fmaxf(fmaxf(S[0], S[1]), S[2]);
#pragma unroll
  for (int i = 3; i < 15; i += 2) t = fmaxf(fmaxf(t, S[i]), S[i + 1]);
  return xhalf_max(fmaxf(t, S[15]));
}
DI f32x16 qk_tile(bf16x8 kA, unsigned kH, bf16x8 qA, u32x4 qB) {
  f32x16 S;
#pragma unroll
  for (int i = 0; i < 16; ++i) S[i] = 0.f;
  u32x4 kBv = {kH, 0x00003F80u, 0u, 0u};
  S = MFMA32(kA, qA, S);
  S = MFMA32(__builtin_bit_cast(bf16x8, kBv), __builtin_bit_cast(bf16x8, qB), S);
  return S;
}
DI void pv_tile(f32x16& S, bf16x8 v0, bf16x8 v1, f32x16& O) {
#pragma unroll
  for (int i = 0; i < 16; ++i) S[i] = __builtin_amdgcn_exp2f(S[i]);
  u32x4 p0 = {pack_bf16(S[0], S[1]), pack_bf16(S[2], S[3]), pack_bf16(S[4], S[5]), pack_bf16(S[6], S[7])};
  u32x4 p1 = {pack_bf16(S[8], S[9]), pack_bf16(S[10], S[11]), pack_bf16(S[12], S[13]), pack_bf16(S[14], S[15])};
  O = MFMA32(v0, __builtin_bit_cast(bf16x8, p0), O);
  O = MFMA32(v1, __builtin_bit_cast(bf16x8, p1), O);
}

constexpr int AT_STAGE = 2 * 8 * KVREC;

DI void attn_body(const u16* __restrict__ Qb, const char* __restrict__ KV, u16* __restrict__ oT, char* lds,
                  const int b) {
  const int head = (b & 7) * 4 + ((b >> 3) & 3), qblk = b >> 5;
  const int w = __builtin_amdgcn_readfirstlane(threadIdx.x >> 6);
  const int l = threadIdx.x & 63, r = l & 31, h = l >> 5;
  const int qp = w & 3, kvh = w >> 2;
  const int token0 = qblk * 256 + qp * 64 + r;

  const unsigned lds_base = (unsigned)(size_t)(LDS_AS char*)lds;
  const char* kvsrc = KV + (size_t)(head * 64) * KVREC;
  auto stage = [&](int s) {
#pragma unroll
    for (int i = 0; i < 5; ++i) {
      int n = w * 5 + i;
      if (n > 37) n -= 2;
      const int hf = n >= 19 ? 1 : 0, pc = n - hf * 19;
      glds16_asm((unsigned)(l * 16), kvsrc + (hf * 32 + s * 8) * KVREC + pc * 1024,
                 lds_base + (s & 1) * AT_STAGE + hf * (8 * KVREC) + pc * 1024);
    }
  };
  STAMPR(1, 14);
  STAMP(1, 0);
  stage(0);

  bf16x8 qA[2];
  u32x4 qB[2];
  float m[2];
  f32x16 O[2];
#pragma unroll
  for (int q = 0; q < 2; ++q) {
    const u16* qrow = Qb + ((size_t)head * SEQ + token0 + q * 32) * 32;
    qA[q] = *(const bf16x8*)(qrow + 8 * h);
    unsigned q67 = *(const unsigned*)(qrow + 16);
    asm volatile("" : "+v"(qA[q]), "+v"(q67));
    qB[q] = (u32x4){h ? 0u : q67, 0u, 0u, 0u};
    m[q] = 0.f;
#pragma unroll
    for (int i = 0; i < 16; ++i) O[q][i] = 0.f;
  }
  bool failed = false;

  const int koff = kvh * (8 * KVREC) + r * 32 + h * 16;
  const int hoff = kvh * (8 * KVREC) + 2240 + r * 4;
  const int voff = kvh * (8 * KVREC) + 1024 + (h * 19 + (r < 18 ? r : 18)) * 16;

#pragma clang loop unroll(disable)
  for (int s = 0; s < 4; ++s) {
    asm volatile("s_waitcnt vmcnt(0)" ::: "memory");
    __builtin_amdgcn_s_barrier();
    asm volatile("" ::: "memory");
    STAMP(1, 1 + s);
    if (s < 3) stage(s + 1);
    const char* sb = lds + (s & 1) * AT_STAGE;
    bf16x8 kA = *(const bf16x8*)(sb + koff);
    unsigned kH = *(const unsigned*)(sb + hoff);
    if (s == 0) {
#pragma unroll
      for (int q = 0; q < 2; ++q) {
        f32x16 S0 = qk_tile(kA, kH, qA[q], qB[q]);
        m[q] = bf16r(tile_max(S0));
        qB[q][1] = h ? 0u : pack_bf16(-m[q], 0.f);
      }
    }
    f32x16 Sa = qk_tile(kA, kH, qA[0], qB[0]);
#pragma unroll
    for (int i = 0; i < 8; ++i) {
      bf16x8 v0 = *(const bf16x8*)(sb + voff + i * KVREC);
      bf16x8 v1 = *(const bf16x8*)(sb + voff + i * KVREC + 608);
      f32x16 Sb = qk_tile(kA, kH, qA[1], qB[1]);
      if (i < 7) {
        kA = *(const bf16x8*)(sb + koff + (i + 1) * KVREC);
        kH = *(const unsigned*)(sb + hoff + (i + 1) * KVREC);
      }
      pv_tile(Sa, v0, v1, O[0]);
      if (i < 7) Sa = qk_tile(kA, kH, qA[0], qB[0]);
      pv_tile(Sb, v0, v1, O[1]);
    }
    float lmax = h ? 0.f : fmaxf(O[0][10], O[1][10]);
    if (__any(!(lmax < 4294967296.f) || !(O[0][10] == O[0][10]) || !(O[1][10] == O[1][10]))) {
#pragma unroll
      for (int q = 0; q < 2; ++q) {
        float lv = O[q][10];
        auto rr = __builtin_amdgcn_permlane32_swap(__float_as_uint(lv), __float_as_uint(lv), false, false);
        lv = __uint_as_float(rr[0]);
        if (__any(!(lv < 1.0e24f))) failed = true;
        float sh = lv > 1.f && lv < 1.0e24f ? floorf(__builtin_amdgcn_logf(lv)) : 0.f;
        float mn = bf16r(m[q] + sh);
        float sc = __builtin_amdgcn_exp2f(m[q] - mn);
        m[q] = mn;
#pragma unroll
        for (int i = 0; i < 16; ++i) O[q][i] *= sc;
        qB[q][1] = h ? 0u : pack_bf16(-mn, 0.f);
      }
    }
  }

  STAMP(1, 5);
  if (failed) {
#pragma clang loop unroll(disable)
    for (int q = 0; q < 2; ++q) {
      const char* rec = KV + (size_t)(head * 64 + kvh * 32) * KVREC;
      const bf16x8 qa = q ? qA[1] : qA[0];
      u32x4 qb = q ? qB[1] : qB[0];
      qb[1] = 0u;
      float mm = -3.0e38f;
      f32x16 Oo;
#pragma unroll
      for (int i = 0; i < 16; ++i) Oo[i] = 0.f;
#pragma clang loop unroll(disable)
      for (int it = 0; it < 32; ++it, rec += KVREC) {
        bf16x8 kA = *(const bf16x8*)(rec + r * 32 + h * 16);
        unsigned kH = *(const unsigned*)(rec + 2240 + r * 4);
        bf16x8 v0 = *(const bf16x8*)(rec + 1024 + (h * 19 + (r < 18 ? r : 18)) * 16);
        bf16x8 v1 = *(const bf16x8*)(rec + 1024 + 608 + (h * 19 + (r < 18 ? r : 18)) * 16);
        f32x16 S = qk_tile(kA, kH, qa, qb);
        float mn = fmaxf(mm, tile_max(S));
        float sc = __builtin_amdgcn_exp2f(mm - mn);
        mm = mn;
#pragma unroll
        for (int i = 0; i < 16; ++i) { S[i] -= mn; Oo[i] *= sc; }
        pv_tile(S, v0, v1, Oo);
      }
      if (q) { O[1] = Oo; m[1] = mm; } else { O[0] = Oo; m[0] = mm; }
    }
  }

  __syncthreads();
  float* mrg = (float*)lds;
  {
    const f32x16 Os = kvh ? O[0] : O[1];
    const float ms = kvh ? m[0] : m[1];
#pragma unroll
    for (int i = 0; i < 12; ++i) mrg[(w * 13 + i) * 64 + l] = Os[i];
    mrg[(w * 13 + 12) * 64 + l] = ms;
  }
  __syncthreads();
  {
    const f32x16 Ok = kvh ? O[1] : O[0];
    const float mk = kvh ? m[1] : m[0];
    const int pw = w ^ 4;
    float m1 = mrg[(pw * 13 + 12) * 64 + l];
    float mm = fmaxf(mk, m1);
    float a0 = __builtin_amdgcn_exp2f(mk - mm), a1 = __builtin_amdgcn_exp2f(m1 - mm);
    float o[12];
#pragma unroll
    for (int i = 0; i < 12; ++i) o[i] = Ok[i] * a0 + mrg[(pw * 13 + i) * 64 + l] * a1;
    float lsum = __shfl(o[10], r);
    float inv = 1.0f / lsum;
    const int token = token0 + kvh * 32;
#pragma unroll
    for (int pr = 0; pr < 5; ++pr) {
      if (pr == 4 && h) continue;
      int d = (pr >> 1) * 8 + (pr & 1) * 2 + 4 * h;
      int c = head * 18 + d;
      unsigned v = pack_bf16(o[2 * pr] * inv, o[2 * pr + 1] * inv);
      *(unsigned*)(oT + ((size_t)(c >> 3) * SEQ + token) * 8 + (c & 7)) = v;
    }
  }
  STAMP(1, 6);
  STAMPR(1, 15);
}

template <int N> DI void wait_vm() { asm volatile("s_waitcnt vmcnt(%0)" ::"i"(N) : "memory"); }
DI void raw_barrier() {
  __builtin_amdgcn_s_barrier();
  asm volatile("" ::: "memory");
}
constexpr int DN_AB = 16384;
constexpr int DN_STG = DN_AB + 18432;

DI void down_body(const u16* __restrict__ hT, const u16* __restrict__ wd, u16* __restrict__ part, char* lds,
                  const int b) {
  const int xcd = b & 7, idx = b >> 3;
  const int ks = xcd >> 1;
  const int bm = (xcd & 1) * 8 + (idx >> 2);
  const int bn = idx & 3;
  const int w = __builtin_amdgcn_readfirstlane(threadIdx.x >> 6);
  const int l = threadIdx.x & 63, g = l >> 4;
  STAMPR(2, 14);
  STAMP(2, 0);
  int tl = 18;
  asm volatile("" : "+s"(tl));
  if (w >= 4) {
    const int lw = w - 4;
    const unsigned lds_base = (unsigned)(size_t)(LDS_AS char*)lds;
    const char* abase = (const char*)(hT + (size_t)(ks * 144) * SEQ * 8);
    const char* wbase = (const char*)(wd + (size_t)(ks * 1152) * DM);
    unsigned avo[4], wvo[5];
#pragma unroll
    for (int i = 0; i < 4; ++i) {
      int off = (lw * 4 + i) * 64 + l;
      avo[i] = (unsigned)(((off >> 7) * SEQ + bm * 128 + (off & 127)) * 16);
    }
#pragma unroll
    for (int i = 0; i < 5; ++i) {
      int n = lw + 4 * i;
      if (n >= 18) n -= 2;
      int c = n * 64 + l;
      int lr = c / 18, ch = c - lr * 18;
      int krow = lr ^ ((lr & 8) >> 1);
      wvo[i] = (unsigned)((krow * DM + bn * 144 + ch * 8) * 2);
    }
    auto issue = [&](int t) {
      const unsigned sb = lds_base + (t & 1) * DN_STG;
#pragma unroll
      for (int i = 0; i < 4; ++i) glds16_asm(avo[i], abase + (size_t)t * (8 * SEQ * 16), sb + (lw * 4 + i) * 1024);
#pragma unroll
      for (int i = 0; i < 5; ++i) {
        int n = lw + 4 * i;
        if (n >= 18) n -= 2;
        glds16_asm(wvo[i], wbase + (size_t)t * (64 * DM * 2), sb + DN_AB + n * 1024);
      }
    };
    issue(0);
    issue(1);
#pragma clang loop unroll(disable)
    for (int t = 0; t < tl; ++t) {
      if (t == 0) wait_vm<9>(); else wait_vm<0>();
      raw_barrier();
      if (t >= 1 && t < 17) issue(t + 1);
    }
  } else {
    const int aoff = g * 2048 + (w * 32 + (l & 15)) * 16;
    const int wq = (l & 15) >> 2, wp = l & 3;
    const int woff0 = DN_AB + (g * 8 + 4 * (0 ^ (g & 1)) + wq) * 288 + wp * 8;
    const int woff1 = DN_AB + (g * 8 + 4 * (1 ^ (g & 1)) + wq) * 288 + wp * 8;
    f32x4 acc[9][2];
#pragma unroll
    for (int i = 0; i < 9; ++i) { acc[i][0] = (f32x4){0.f, 0.f, 0.f, 0.f}; acc[i][1] = (f32x4){0.f, 0.f, 0.f, 0.f}; }
#pragma clang loop unroll(disable)
    for (int t = 0; t < tl; ++t) {
      raw_barrier();
      const char* sb = lds + (t & 1) * DN_STG;
      auto load_w = [&](int grp) -> bf16x8 {
        const int kk = grp / 9, ct = grp - kk * 9;
        s16x4 lo = tr_read(sb + woff0 + kk * 9216 + ct * 32);
        s16x4 hi = tr_read(sb + woff1 + kk * 9216 + ct * 32);
        return __builtin_shufflevector(lo, hi, 0, 1, 2, 3, 4, 5, 6, 7);
      };
      bf16x8 af[2][2], wq_[3];
      wq_[0] = load_w(0);
#pragma unroll
      for (int kk = 0; kk < 2; ++kk)
#pragma unroll
        for (int rt = 0; rt < 2; ++rt) af[kk][rt] = *(const bf16x8*)(sb + aoff + kk * 8192 + rt * 256);
      wq_[1] = load_w(1);
      wq_[2] = load_w(2);
#pragma unroll
      for (int grp = 0; grp < 18; ++grp) {
        bf16x8 wf = wq_[0];
        wq_[0] = wq_[1];
        wq_[1] = wq_[2];
        if (grp + 3 < 18) wq_[2] = load_w(grp + 3);
        const int kk = grp / 9, ct = grp - kk * 9;
        acc[ct][0] = MFMA16(wf, af[kk][0], acc[ct][0]);
        acc[ct][1] = MFMA16(wf, af[kk][1], acc[ct][1]);
        __builtin_amdgcn_sched_barrier(0);
      }
    }
    STAMP(2, 2);
#pragma unroll
    for (int ct = 0; ct < 9; ++ct)
#pragma unroll
      for (int rt = 0; rt < 2; ++rt) {
        int tt = bm * 8 + w * 2 + rt, ctg = bn * 9 + ct;
        u32x2 pv = {pack_bf16(acc[ct][rt][0], acc[ct][rt][1]), pack_bf16(acc[ct][rt][2], acc[ct][rt][3])};
        *(u32x2*)(part + ((size_t)(ks * 128 + tt) * 36 + ctg) * 256 + l * 4) = pv;
      }
    STAMP(2, 3);
    STAMPR(2, 15);
  }
}

__global__ __launch_bounds__(512, 4) void mid_kernel(const u16* __restrict__ Qb, const char* __restrict__ KV,
                                                     u16* __restrict__ oT, const u16* __restrict__ hT,
                                                     const u16* __restrict__ wd, u16* __restrict__ part) {
  __shared__ __attribute__((aligned(16))) char lds[2 * AT_STAGE];
  const int b = blockIdx.x;
  if (b >= 256) down_body(hT, wd, part, lds, b - 256);
  else attn_body(Qb, KV, oT, lds, b);
}

__global__ __launch_bounds__(256, 2) void final_kernel(const u16* __restrict__ oT, const u16* __restrict__ wo,
                                                       const u16* __restrict__ part, const float* __restrict__ gin,
                                                       u16* __restrict__ out) {
  __shared__ __attribute__((aligned(16))) char lds[576 * 96 + 2048];
  const int b = blockIdx.x;
  const int idx = b >> 3;
  const int bm = (b & 7) * 4 + idx / 12, bn = idx % 12;
  const int w = __builtin_amdgcn_readfirstlane(threadIdx.x >> 6);
  const int l = threadIdx.x & 63, g = l >> 4;
  STAMPR(3, 14);
  STAMP(3, 0);
  const unsigned lds_base = (unsigned)(size_t)(LDS_AS char*)lds;
#pragma unroll
  for (int i = 0; i < 14; ++i) {
    const int n = w * 14 + i;
    int c = n * 64 + l;
    int lr = c / 6, ch = c - lr * 6;
    if (lr > 575) lr = 575;
    int krow = lr ^ ((lr & 8) >> 1);
    glds16_asm((unsigned)((krow * DM + bn * 48 + ch * 8) * 2), wo, lds_base + n * 1024);
  }
  const int token = bm * 64 + w * 16 + (l & 15);
  bf16x8 af[18];
#pragma unroll
  for (int ks = 0; ks < 18; ++ks) af[ks] = *(const bf16x8*)(oT + ((size_t)(ks * 4 + g) * SEQ + token) * 8);
  float gg = gin[token];
  const int tt = bm * 4 + w;
  f32x4 bsum[3];
#pragma unroll
  for (int ct = 0; ct < 3; ++ct) {
    int ctg = bn * 3 + ct;
    f32x4 pp[4];
#pragma unroll
    for (int ks = 0; ks < 4; ++ks) {
      u32x2 v = *(const u32x2*)(part + ((size_t)(ks * 128 + tt) * 36 + ctg) * 256 + l * 4);
      pp[ks] = (f32x4){__uint_as_float(v[0] << 16), __uint_as_float(v[0] & 0xffff0000u),
                       __uint_as_float(v[1] << 16), __uint_as_float(v[1] & 0xffff0000u)};
    }
    bsum[ct] = (pp[0] + pp[1]) + (pp[2] + pp[3]);
  }
  STAMP(3, 1);
  asm volatile("s_waitcnt vmcnt(0)" ::: "memory");
  __builtin_amdgcn_s_barrier();
  asm volatile("" ::: "memory");
  STAMP(3, 2);
  const int wq = (l & 15) >> 2, wp = l & 3;
  const int woff0 = (g * 8 + 4 * (0 ^ (g & 1)) + wq) * 96 + wp * 8;
  const int woff1 = (g * 8 + 4 * (1 ^ (g & 1)) + wq) * 96 + wp * 8;
  f32x4 acc[3];
#pragma unroll
  for (int i = 0; i < 3; ++i) acc[i] = (f32x4){0.f, 0.f, 0.f, 0.f};
#pragma unroll
  for (int ks = 0; ks < 18; ++ks)
#pragma unroll
    for (int ct = 0; ct < 3; ++ct) {
      s16x4 lo = tr_read(lds + woff0 + ks * 3072 + ct * 32);
      s16x4 hi = tr_read(lds + woff1 + ks * 3072 + ct * 32);
      bf16x8 wf = __builtin_shufflevector(lo, hi, 0, 1, 2, 3, 4, 5, 6, 7);
      acc[ct] = MFMA16(wf, af[ks], acc[ct]);
    }
  STAMP(3, 3);
  const float og = bf16r(1.0f - gg);
#pragma unroll
  for (int ct = 0; ct < 3; ++ct) {
    int ctg = bn * 3 + ct;
    float o[4];
#pragma unroll
    for (int e = 0; e < 4; ++e) o[e] = bf16r(gg * bf16r(acc[ct][e])) + bf16r(og * bf16r(bsum[ct][e]));
    u32x2 ov = {pack_bf16(o[0], o[1]), pack_bf16(o[2], o[3])};
    *(u32x2*)(out + (size_t)token * DM + ctg * 16 + g * 4) = ov;
  }
  STAMP(3, 4);
  STAMPR(3, 15);
}

extern "C" void kernel_launch(void* const* d_in, const int* in_sizes, int n_in, void* d_out, int out_size,
                              void* d_ws, size_t ws_size, hipStream_t stream) {
  const u16* x = (const u16*)d_in[0];
  const u16* wqkv = (const u16*)d_in[1];
  const u16* wo = (const u16*)d_in[2];
  const u16* wg = (const u16*)d_in[4];
  const u16* wu = (const u16*)d_in[5];
  const u16* wd = (const u16*)d_in[6];
  const u16* gw = (const u16*)d_in[7];
  char* ws = (char*)d_ws;
  u16* hT = (u16*)(ws + WS_HT);
  u16* Qb = (u16*)(ws + WS_QB);
  char* KV = ws + WS_KV;
  u16* oT = (u16*)(ws + WS_OT);
  u16* part = (u16*)(ws + WS_PART);
  float* gbuf = (float*)(ws + WS_G);
  u16* out = (u16*)d_out;

  gemm1_kernel<<<256, 512, 0, stream>>>(x, wg, wu, wqkv, hT, Qb, KV, gw, gbuf, wd, wo);
  mid_kernel<<<512, 512, 0, stream>>>(Qb, KV, oT, hT, wd, part);
  final_kernel<<<384, 256, 0, stream>>>(oT, wo, part, gbuf, out);
}
```
